```python
import torch

def _softmax(x, axis=-1):
    return torch.softmax(x, dim=axis)
def _gelu(x):
    return torch.nn.functional.gelu(x, approximate="tanh")
def _silu(x):
    return torch.nn.functional.silu(x)
def _sigmoid(x):
    return torch.sigmoid(x)
def _layernorm(x, axis=-1, eps=1e-5):
    m = x.mean(dim=axis, keepdim=True)
    v = x.var(dim=axis, keepdim=True, unbiased=False)
    return (x - m) / torch.sqrt(v + eps)
def _scatter_add(tgt, idx, src, axis=0):
    return tgt.clone().index_add_(axis, idx, src)
def _segment_sum(src, seg, n):
    out = torch.zeros((n,) + src.shape[1:], dtype=src.dtype)
    return out.index_add_(0, seg, src)
def _pad(x, pw):
    flat = tuple(v for pair in reversed(pw) for v in pair)
    return torch.nn.functional.pad(x, flat)

kernel_name = 'sequential_preLN__mha_block__mixer_channel_mlp__prompt'

def setup_inputs():

    _w = (lambda a: (lambda t: t.to(torch.bfloat16) if t.is_floating_point() else t)(torch.as_tensor(a)))
    def _A_setup():
        B, S, D, H = 1, 2048, 640, 32
        return {
            "x": _w((lambda *s: torch.randn(*s, dtype=torch.bfloat16) * 0.05)(B, S, D)),
            "wqkv": _w((lambda *s: torch.randn(*s, dtype=torch.bfloat16) * 0.05)(D, 3 * D)),
            "wo": _w((lambda *s: torch.randn(*s, dtype=torch.bfloat16) * 0.05)(D, D)),
        }
    _A_in = _A_setup()

    def _B_setup():
        B, S, D, F = 1, 2048, 640, 1728
        return {
            "x": _w((lambda *s: torch.randn(*s, dtype=torch.bfloat16) * 0.05)(B, S, D)),
            "w1": _w((lambda *s: torch.randn(*s, dtype=torch.bfloat16) * 3.2)(D, F) * (D ** -0.5)),
            "b1": _w((lambda *s: torch.randn(*s, dtype=torch.bfloat16) * 1.6)(F)),
            "w2": _w((lambda *s: torch.randn(*s, dtype=torch.bfloat16) * 0.2)(F, D) * (F ** -0.5)),
            "b2": _w((lambda *s: torch.randn(*s, dtype=torch.bfloat16) * 0.4)(D)),
        }
    _B_in = _B_setup()

    return {
        "A_x": _A_in["x"],
        "A_wqkv": _A_in["wqkv"],
        "A_wo": _A_in["wo"],
        "B_x": _B_in["x"],
        "B_w1": _B_in["w1"],
        "B_b1": _B_in["b1"],
        "B_w2": _B_in["w2"],
        "B_b2": _B_in["b2"],
    }

def reference(A_x, A_wqkv, A_wo, B_x, B_w1, B_b1, B_w2, B_b2):
    def _A_ref(x, wqkv, wo):
        B, S, D = x.shape
        H = 32
        Dh = D // H
        qkv = (lambda x, *d: x.permute(*d))((x @ wqkv).reshape(B, S, 3, H, Dh), 2, 0, 3, 1, 4)
        q, k, v = qkv[0], qkv[1], qkv[2]
        scores = torch.einsum("bhqd,bhkd->bhqk", q, k) * (Dh ** -0.5)
        p = _softmax(scores, axis=-1)
        o = torch.einsum("bhqk,bhkd->bhqd", p, v)
        return (lambda x, *d: x.permute(*d))(o, 0, 2, 1, 3).reshape(B, S, D) @ wo

    def _B_ref(x, w1, b1, w2, b2):
        y = _layernorm(x, axis=-1)
        h = _gelu(torch.einsum("bsd,df->bsf", y, w1) + b1)
        return torch.einsum("bsf,fd->bsd", h, w2) + b2

    x = A_x
    h = x + _A_ref(_layernorm(x, axis=-1), A_wqkv, A_wo)
    h2 = h + _B_ref(_layernorm(h, axis=-1), B_w1, B_b1, B_w2, B_b2)
    return h2
```

```cpp
#include <hip/hip_runtime.h>
#include <hip/hip_bf16.h>
#include <stdint.h>
namespace naive {
constexpr int S = 2048, D = 640, H = 32, DH = 20, F = 1728;
__device__ __forceinline__ float bf2f(uint16_t b) { return __uint_as_float((uint32_t)b << 16); }
__device__ __forceinline__ uint16_t f2bf(float f) { uint32_t u = __float_as_uint(f); u += 0x7FFFu + ((u >> 16) & 1u); return (uint16_t)(u >> 16); }
__device__ __forceinline__ float rbf(float f) { return bf2f(f2bf(f)); }
__global__ void cvt(const uint16_t* in, float* out, int n) { int i = blockIdx.x * 256 + threadIdx.x; if (i < n) out[i] = bf2f(in[i]); }
__global__ void ln(const float* in, float* out) {
    int r = blockIdx.x, t = threadIdx.x; float v[10]; float s = 0;
    for (int i = 0; i < 10; ++i) { v[i] = in[r * D + t + 64 * i]; s += v[i]; }
    for (int o = 32; o; o >>= 1) s += __shfl_xor(s, o);
    float m = s / D, q = 0;
    for (int i = 0; i < 10; ++i) { float d = v[i] - m; q += d * d; }
    for (int o = 32; o; o >>= 1) q += __shfl_xor(q, o);
    float rs = 1.0f / sqrtf(q / D + 1e-5f);
    for (int i = 0; i < 10; ++i) out[r * D + t + 64 * i] = (v[i] - m) * rs;
}
__global__ void gemm(const float* A, const uint16_t* B, float* C, int M, int N, int K) {
    __shared__ float As[16][17], Bs[16][17];
    int tx = threadIdx.x & 15, ty = threadIdx.x >> 4; int m = blockIdx.y * 16 + ty, n = blockIdx.x * 16 + tx; float acc = 0;
    for (int k0 = 0; k0 < K; k0 += 16) {
        As[ty][tx] = A[(size_t)m * K + k0 + tx]; Bs[ty][tx] = bf2f(B[(size_t)(k0 + ty) * N + n]);
        __syncthreads();
        for (int k = 0; k < 16; ++k) acc += As[ty][k] * Bs[k][tx];
        __syncthreads();
    }
    C[(size_t)m * N + n] = acc;
}
__global__ void attn(const float* qkv, float* o) {
    int q = blockIdx.x * 64 + threadIdx.x, h = blockIdx.y; float qv[DH], acc[DH];
    for (int d = 0; d < DH; ++d) { qv[d] = qkv[(size_t)q * 3 * D + h * DH + d] * 0.22360679774997896f; acc[d] = 0; }
    float m = -INFINITY, l = 0;
    for (int k = 0; k < S; ++k) {
        float s = 0; for (int d = 0; d < DH; ++d) s += qv[d] * qkv[(size_t)k * 3 * D + D + h * DH + d];
        float mn = fmaxf(m, s), a = expf(m - mn), p = expf(s - mn); l = l * a + p;
        for (int d = 0; d < DH; ++d) acc[d] = acc[d] * a + p * qkv[(size_t)k * 3 * D + 2 * D + h * DH + d];
        m = mn;
    }
    for (int d = 0; d < DH; ++d) o[(size_t)q * D + h * DH + d] = acc[d] / l;
}
__global__ void addk(const float* a, const float* b, float* c, int n) { int i = blockIdx.x * 256 + threadIdx.x; if (i < n) c[i] = a[i] + b[i]; }
__global__ void gelu(float* z, const uint16_t* b1, int n) { int i = blockIdx.x * 256 + threadIdx.x; if (i < n) { float x = z[i] + bf2f(b1[i % F]); z[i] = 0.5f * x * (1.f + tanhf(0.7978845608028654f * (x + 0.044715f * x * x * x))); } }
__global__ void fin(const float* h, const float* y, const uint16_t* b2, uint16_t* out, float* outf, int n) { int i = blockIdx.x * 256 + threadIdx.x; if (i < n) { float v = h[i] + y[i] + bf2f(b2[i % D]); out[i] = f2bf(v); if (outf) outf[i] = v; } }
struct Bufs { float *x, *xn, *qkv, *o, *ao, *h, *y1, *y2, *z, *mo, *outf; };
inline Bufs carve(void* ws) { float* p = (float*)ws; Bufs b; b.x = p; p += S * D; b.xn = p; p += S * D; b.qkv = p; p += S * 3 * D; b.o = p; p += S * D; b.ao = p; p += S * D; b.h = p; p += S * D; b.y1 = p; p += S * D; b.y2 = p; p += S * D; b.z = p; p += S * F; b.mo = p; p += S * D; b.outf = p; p += S * D; return b; }
inline void run(void* const* d_in, void* d_out, void* ws, hipStream_t st) {
    const uint16_t *x = (const uint16_t*)d_in[0], *wqkv = (const uint16_t*)d_in[1], *wo = (const uint16_t*)d_in[2], *w1 = (const uint16_t*)d_in[4], *b1 = (const uint16_t*)d_in[5], *w2 = (const uint16_t*)d_in[6], *b2 = (const uint16_t*)d_in[7];
    Bufs b = carve(ws); int n = S * D;
    cvt<<<n / 256, 256, 0, st>>>(x, b.x, n);
    ln<<<S, 64, 0, st>>>(b.x, b.xn);
    gemm<<<dim3(3 * D / 16, S / 16), 256, 0, st>>>(b.xn, wqkv, b.qkv, S, 3 * D, D);
    attn<<<dim3(S / 64, H), 64, 0, st>>>(b.qkv, b.o);
    gemm<<<dim3(D / 16, S / 16), 256, 0, st>>>(b.o, wo, b.ao, S, D, D);
    addk<<<n / 256, 256, 0, st>>>(b.x, b.ao, b.h, n);
    ln<<<S, 64, 0, st>>>(b.h, b.y1);
    ln<<<S, 64, 0, st>>>(b.y1, b.y2);
    gemm<<<dim3(F / 16, S / 16), 256, 0, st>>>(b.y2, w1, b.z, S, F, D);
    gelu<<<S * F / 256, 256, 0, st>>>(b.z, b1, S * F);
    gemm<<<dim3(D / 16, S / 16), 256, 0, st>>>(b.z, w2, b.mo, S, D, F);
    fin<<<n / 256, 256, 0, st>>>(b.h, b.mo, b2, (uint16_t*)d_out, b.outf, n);
}
}
extern "C" void kernel_launch(void* const* d_in, const int* in_sizes, int n_in, void* d_out, int out_size, void* d_ws, size_t ws_size, hipStream_t stream) {
    naive::run(d_in, d_out, d_ws, stream);
}
```

```cpp
#include <hip/hip_runtime.h>
#include <stdint.h>

namespace {
constexpr int S = 2048, D = 640, NH = 32, DH = 20, FF = 1728;
typedef __attribute__((ext_vector_type(16))) float f32x16;
typedef __attribute__((ext_vector_type(8))) float f32x8;
typedef __attribute__((ext_vector_type(4))) float f32x4;
typedef __attribute__((ext_vector_type(2))) float f32x2;
typedef __attribute__((ext_vector_type(8))) __bf16 bf16x8;
typedef __attribute__((ext_vector_type(16))) __bf16 bf16x16;
typedef __attribute__((ext_vector_type(2))) __bf16 bf16x2;
typedef __attribute__((ext_vector_type(4))) short s16x4;
typedef __attribute__((ext_vector_type(4))) unsigned u32x4;
typedef __attribute__((ext_vector_type(2))) unsigned u32x2;
#define LAS __attribute__((address_space(3)))
typedef LAS char* lds_ptr;

constexpr int CHUNK = 21504;
constexpr int QCH = 10240;
constexpr size_t WS_KV = 0;
constexpr size_t WS_Q = WS_KV + (size_t)NH * 8 * CHUNK;
constexpr size_t WS_O = WS_Q + (size_t)NH * 8 * QCH;
constexpr size_t WS_H = WS_O + (size_t)S * D * 2;
constexpr size_t WS_H1 = WS_H + (size_t)S * D * 2;
constexpr size_t WS_END = WS_H1 + (size_t)S * FF * 2;

#define STAMP(wsb, kid, idx)
__device__ __forceinline__ void dma16(const void* sbase, unsigned voff, unsigned ldsaddr) {
    unsigned keep;
    asm volatile("s_mov_b32 %0, m0\n\ts_mov_b32 m0, %3\n\ts_nop 0\n\tglobal_load_lds_dwordx4 %1, %2\n\ts_mov_b32 m0, %0"
                 : "=&s"(keep) : "v"(voff), "s"(sbase), "s"(ldsaddr) : "memory");
}
template <int N> __device__ __forceinline__ void wait_bar() {
    asm volatile("s_waitcnt vmcnt(%0) lgkmcnt(0)\n\ts_barrier" :: "i"(N) : "memory");
}
__device__ __forceinline__ void lds_bar() { asm volatile("s_waitcnt lgkmcnt(0)\n\ts_barrier" ::: "memory"); }
__device__ __forceinline__ u32x4 lds128(lds_ptr p) { return *(LAS const u32x4*)p; }
__device__ __forceinline__ u32x2 ldstr(lds_ptr p) {
    return __builtin_bit_cast(u32x2, __builtin_amdgcn_ds_read_tr16_b64_v4i16((LAS s16x4*)p));
}
__device__ __forceinline__ f32x16 mfma32(u32x4 a, u32x4 b, f32x16 c) {
    return __builtin_amdgcn_mfma_f32_32x32x16_bf16(__builtin_bit_cast(bf16x8, a), __builtin_bit_cast(bf16x8, b), c, 0, 0, 0);
}
__device__ __forceinline__ f32x16 mfma8(u32x2 a, u32x2 b, f32x16 c) {
    return __builtin_amdgcn_mfma_f32_32x32x8bf16_1k(__builtin_bit_cast(s16x4, a), __builtin_bit_cast(s16x4, b), c, 0, 0, 0);
}
__device__ __forceinline__ float dot2(unsigned a, unsigned b, float c) {
    return __builtin_amdgcn_fdot2_f32_bf16(__builtin_bit_cast(bf16x2, a), __builtin_bit_cast(bf16x2, b), c, false);
}
__device__ __forceinline__ unsigned pk2(float lo, float hi) {
    bf16x2 v = {(__bf16)lo, (__bf16)hi};
    return __builtin_bit_cast(unsigned, v);
}
__device__ __forceinline__ float bflo(unsigned u) { return __uint_as_float(u << 16); }
__device__ __forceinline__ float bfhi(unsigned u) { return __uint_as_float(u & 0xffff0000u); }
__device__ __forceinline__ float xhalf(float v) {
    return __shfl_xor(v, 32);
}

constexpr int LN_NST = 4, LN_STAGE = 32768;

template <bool CT>
__device__ __forceinline__ void ln_mainloop(const char* A, const char* W, int ldw2, lds_ptr lds, unsigned lds0, int w, int lane,
                                            const unsigned (&va)[4], unsigned vb, int rot,
                                            f32x16 (&acc)[2][2], float (&xs)[2], float (&xq)[2], float (&ws)[2]) {
    const int wm = w >> 1, wn = w & 1, hh = lane >> 5;
    auto issue = [&](int t) {
        int kt = t + rot; if (kt >= 10) kt -= 10;
        const unsigned slot = lds0 + (t % LN_NST) * LN_STAGE;
        const char* ab = A + kt * 128;
        const char* wb = W + (size_t)kt * 64 * ldw2;
#pragma unroll
        for (int p = 0; p < 4; ++p) dma16(ab, va[p], slot + (w * 4 + p) * 1024);
#pragma unroll
        for (int p = 0; p < 4; ++p) dma16(wb + (size_t)p * 4 * ldw2, vb, slot + 16384 + (w * 4 + p) * 1024);
    };
    unsigned aoff[4], boff[4];
    const int kx = 2 * wn + wm;
#pragma unroll
    for (int ks = 0; ks < 4; ++ks) {
        const int k2 = ks ^ kx;
        aoff[ks] = (wm * 64 + (lane & 31)) * 128 + (((2 * k2 + hh) ^ ((lane >> 1) & 7)) << 4);
        boff[ks] = 16384 + (4 * k2 + 2 * hh) * 1024 + wn * 512 + ((lane & 15) >> 2) * 64 + ((lane >> 4) & 1) * 32 + (lane & 3) * 8;
    }
#pragma unroll
    for (int t = 0; t < LN_NST - 1; ++t) issue(t);
#pragma unroll
    for (int t = 0; t < 10; ++t) {
        const int ahead = (10 - 1 - t) < (LN_NST - 2) ? (10 - 1 - t) : (LN_NST - 2);
        if (ahead == 2) wait_bar<16>(); else if (ahead == 1) wait_bar<8>(); else if (ahead == 3) wait_bar<24>(); else wait_bar<0>();
        if (t + LN_NST - 1 < 10) issue(t + LN_NST - 1);
        lds_ptr sb = lds + (t % LN_NST) * LN_STAGE;
#pragma unroll
        for (int ks = 0; ks < 4; ++ks) {
            u32x4 xa[2], wf[2];
#pragma unroll
            for (int i = 0; i < 2; ++i) xa[i] = lds128(sb + aoff[ks] + i * 4096);
#pragma unroll
            for (int j = 0; j < 2; ++j) {
                u32x2 lo = ldstr(sb + boff[ks] + j * 256), hi = ldstr(sb + boff[ks] + j * 256 + 1024);
                wf[j] = (u32x4){lo[0], lo[1], hi[0], hi[1]};
            }
#pragma unroll
            for (int i = 0; i < 2; ++i)
#pragma unroll
                for (int j = 0; j < 2; ++j) acc[i][j] = CT ? mfma32(wf[j], xa[i], acc[i][j]) : mfma32(xa[i], wf[j], acc[i][j]);
            if (ks < 2) {
#pragma unroll
                for (int i = 0; i < 2; ++i)
#pragma unroll
                    for (int e = 0; e < 4; ++e) { xs[i] = dot2(xa[i][e], 0x3f803f80u, xs[i]); xq[i] = dot2(xa[i][e], xa[i][e], xq[i]); }
            }
            if ((ks & 1) == 0) {
#pragma unroll
                for (int j = 0; j < 2; ++j)
#pragma unroll
                    for (int e = 0; e < 4; ++e) ws[j] = dot2(wf[j][e], 0x3f803f80u, ws[j]);
            }
        }
    }
}

template <int MODE>
__global__ __launch_bounds__(256, 1) void k_gemm_ln(const char* __restrict__ A, const char* __restrict__ W, const uint16_t* __restrict__ bias,
                                                    char* __restrict__ ws_base) {
    extern __shared__ __attribute__((aligned(16))) char smem[];
    constexpr int N = MODE == 0 ? 3 * D : FF, NTN = MODE == 0 ? 15 : 14, PER = MODE == 0 ? 30 : 28;
    const int tid = threadIdx.x, lane = tid & 63, w = __builtin_amdgcn_readfirstlane(tid >> 6);
    const int wm = w >> 1, wn = w & 1, hh = lane >> 5, l31 = lane & 31;
    STAMP(ws_base, MODE == 0 ? 0 : 3, 0);
    const int L = (blockIdx.x & 7) * PER + (blockIdx.x >> 3);
    const int grp = L / (4 * NTN), rem = L % (4 * NTN);
    const int mt = grp * 4 + (rem & 3), nt = rem >> 2;
    const int m0 = mt * 128, n0 = nt * 128;
    lds_ptr lds = (lds_ptr)smem;
    const unsigned lds0 = (unsigned)(uintptr_t)smem;
    unsigned va[4], vb;
#pragma unroll
    for (int p = 0; p < 4; ++p) {
        const int row = w * 32 + p * 8 + (lane >> 3);
        va[p] = (m0 + row) * (D * 2) + (((lane & 7) ^ ((row >> 1) & 7)) << 4);
    }
    {
        int col = n0 + (lane >> 4) * 32 + (lane & 3) * 8;
        if (col > N - 8) col = N - 8;
        vb = (w * 16 + ((lane >> 2) & 3)) * (N * 2) + col * 2;
    }
    f32x16 acc[2][2];
#pragma unroll
    for (int i = 0; i < 2; ++i)
#pragma unroll
        for (int j = 0; j < 2; ++j) acc[i][j] = f32x16{};
    float xs[2] = {0.f, 0.f}, xq[2] = {0.f, 0.f}, cs[2] = {0.f, 0.f};
    const bool vtile = (MODE == 0) && (nt >= 10);
    const int rot = 0;
    if (vtile) ln_mainloop<false>(A, W, N * 2, lds, lds0, w, lane, va, vb, rot, acc, xs, xq, cs);
    else ln_mainloop<true>(A, W, N * 2, lds, lds0, w, lane, va, vb, rot, acc, xs, xq, cs);

    STAMP(ws_base, MODE == 0 ? 0 : 3, 1);
    LAS float* px = (LAS float*)(lds + 2 * LN_STAGE);
    LAS float* pw = px + 512;
    LAS float* mr = pw + 256;
    LAS float* cw = mr + 256;
#pragma unroll
    for (int i = 0; i < 2; ++i) {
        const float a = xs[i] + xhalf(xs[i]), b = xq[i] + xhalf(xq[i]), c = cs[i] + xhalf(cs[i]);
        if (hh == 0) {
            *(LAS f32x2*)(px + (wn * 128 + wm * 64 + i * 32 + l31) * 2) = (f32x2){a, b};
            pw[wm * 128 + wn * 64 + i * 32 + l31] = c;
        }
    }
    lds_bar();
    if (tid < 128) {
        const float sum = px[tid * 2] + px[256 + tid * 2], sq = px[tid * 2 + 1] + px[256 + tid * 2 + 1];
        const float mean = sum * (1.0f / D);
        const float var = fmaxf(sq * (1.0f / D) - mean * mean, 0.f);
        float r = __builtin_amdgcn_rsqf(var + 1e-5f);
        if (MODE == 1) r *= __builtin_amdgcn_rsqf(var * r * r + 1e-5f);
        mr[tid] = mean; mr[128 + tid] = r;
    } else {
        cw[tid - 128] = pw[tid - 128] + pw[tid];
    }
    lds_bar();

    STAMP(ws_base, MODE == 0 ? 0 : 3, 2);
    if (MODE == 0) {
        if (!vtile) {
            const bool isq = nt < 5;
            char* base = ws_base + (isq ? WS_Q : WS_KV);
            const int cstride = isq ? QCH : CHUNK;
            const float sc = isq ? 0.22360679774997896f * 1.4426950408889634f : 1.0f;
#pragma unroll
            for (int i = 0; i < 2; ++i) {
                const int rl = wm * 64 + i * 32 + l31, tok = m0 + rl;
                const float mean = mr[rl], r = mr[128 + rl] * sc;
                const int tmain = (tok >> 8) * cstride + ((tok >> 5) & 7) * 1024 + l31 * 16;
                const int trest = (tok >> 8) * cstride + 8192 + ((tok >> 7) & 1) * 1024 + (tok & 63) * 16 + ((tok >> 6) & 1) * 8;
#pragma unroll
                for (int j = 0; j < 2; ++j)
#pragma unroll
                    for (int g = 0; g < 4; ++g) {
                        const int nl = wn * 64 + j * 32 + g * 8 + hh * 4;
                        const f32x4 c4 = *(LAS const f32x4*)(cw + nl);
                        const int nn = n0 - (isq ? 0 : D) + nl;
                        const int head = (nn * 205) >> 12, d0 = nn - head * 20;
                        float v[4];
#pragma unroll
                        for (int e = 0; e < 4; ++e) v[e] = r * (acc[i][j][4 * g + e] - mean * c4[e]);
                        const int off = head * 8 * cstride + (d0 < 16 ? tmain + (d0 >> 3) * 512 + (d0 & 4) * 2 : trest);
                        *(u32x2*)(base + off) = (u32x2){pk2(v[0], v[1]), pk2(v[2], v[3])};
                    }
            }
        } else {
            char* base = ws_base + WS_KV;
#pragma unroll
            for (int j = 0; j < 2; ++j) {
                const int nl = wn * 64 + j * 32 + l31, nn = n0 - 2 * D + nl;
                const int head = (nn * 205) >> 12, d = nn - head * 20;
                const float c = cw[nl];
#pragma unroll
                for (int i = 0; i < 2; ++i) {
                    const int kt = (m0 + wm * 64 + i * 32) >> 5;
                    char* tb = base + (head * 8 + (kt >> 3)) * CHUNK + 10240 + (kt & 7) * 1408;
#pragma unroll
                    for (int g = 0; g < 4; ++g) {
                        const int rl = wm * 64 + i * 32 + g * 8 + hh * 4;
                        const f32x4 m4 = *(LAS const f32x4*)(mr + rl), r4 = *(LAS const f32x4*)(mr + 128 + rl);
                        float v[4];
#pragma unroll
                        for (int e = 0; e < 4; ++e) v[e] = r4[e] * (acc[i][j][4 * g + e] - m4[e] * c);
                        *(u32x2*)(tb + (g >> 1) * 704 + ((g & 1) * 20 + d) * 16 + hh * 8) = (u32x2){pk2(v[0], v[1]), pk2(v[2], v[3])};
                    }
                    if (d == 0) {
                        *(u32x4*)(tb + hh * 704 + 640) = (u32x4){0x3f803f80u, 0x3f803f80u, 0x3f803f80u, 0x3f803f80u};
                        *(u32x4*)(tb + hh * 704 + 656) = (u32x4){0u, 0u, 0u, 0u};
                    }
                }
            }
        }
    } else {
        if (n0 + wn * 64 < N) {
            char* out = ws_base + WS_H1;
#pragma unroll
            for (int i = 0; i < 2; ++i) {
                const int rl = wm * 64 + i * 32 + l31;
                const float mean = mr[rl], r = mr[128 + rl];
                char* orow = out + (size_t)(m0 + rl) * (FF * 2);
#pragma unroll
                for (int j = 0; j < 2; ++j) {
                    u32x2 pk[4];
#pragma unroll
                    for (int g = 0; g < 4; ++g) {
                        const int nl = wn * 64 + j * 32 + g * 8 + hh * 4;
                        const f32x4 c4 = *(LAS const f32x4*)(cw + nl);
                        const u32x2 b2 = *(const u32x2*)(bias + n0 + nl);
                        const float bb[4] = {bflo(b2[0]), bfhi(b2[0]), bflo(b2[1]), bfhi(b2[1])};
                        float v[4];
#pragma unroll
                        for (int e = 0; e < 4; ++e) {
                            const float z = r * (acc[i][j][4 * g + e] - mean * c4[e]) + bb[e];
                            const float t = z * (-2.302208198f - 0.1029432397f * z * z);
                            v[e] = z * __builtin_amdgcn_rcpf(1.0f + __builtin_amdgcn_exp2f(t));
                        }
                        pk[g] = (u32x2){pk2(v[0], v[1]), pk2(v[2], v[3])};
                    }
#pragma unroll
                    for (int g = 0; g < 4; g += 2) {
                        auto r0 = __builtin_amdgcn_permlane32_swap(pk[g][0], pk[g + 1][0], false, false);
                        auto r1 = __builtin_amdgcn_permlane32_swap(pk[g][1], pk[g + 1][1], false, false);
                        *(u32x4*)(orow + (n0 + wn * 64 + j * 32 + g * 8 + hh * 8) * 2) = (u32x4){r0[0], r1[0], r0[1], r1[1]};
                    }
                }
            }
        }
    }
    STAMP(ws_base, MODE == 0 ? 0 : 3, 3);
}

constexpr int RS_NST = 6, RS_STAGE = 20480;

template <int K, bool BIAS>
__global__ __launch_bounds__(256, 1) void k_gemm_res(const char* __restrict__ A, const char* __restrict__ W, const char* __restrict__ res,
                                                     const uint16_t* __restrict__ bias, char* __restrict__ out, char* ws_base) {
    extern __shared__ __attribute__((aligned(16))) char smem[];
    constexpr int NT = K / 64;
    const int tid = threadIdx.x, lane = tid & 63, w = __builtin_amdgcn_readfirstlane(tid >> 6);
    const int kh = w >> 1, nh = w & 1, hh = lane >> 5, l31 = lane & 31;
    STAMP(ws_base, BIAS ? 4 : 2, 0);
    const int x = blockIdx.x & 7, jx = blockIdx.x >> 3;
    const int L = (x < 4 ? x * 28 : 112 + (x - 4) * 27) + jx;
    const int mt = L / 10, nt = L % 10, m0 = mt * 96, n0 = nt * 64;
    lds_ptr lds = (lds_ptr)smem;
    const unsigned lds0 = (unsigned)(uintptr_t)smem;
    u32x4 rv[3]; u32x4 bv = {};
#pragma unroll
    for (int r = 0; r < 3; ++r) {
        const int it = tid + 256 * r; int row = m0 + (it >> 3); if (row > S - 1) row = S - 1;
        rv[r] = *(const u32x4*)(res + (size_t)row * (D * 2) + (n0 + (it & 7) * 8) * 2);
    }
    if (BIAS) bv = *(const u32x4*)(bias + n0 + (tid & 7) * 8);
    unsigned va[3], vb;
#pragma unroll
    for (int p = 0; p < 3; ++p) {
        const int row = w * 24 + p * 8 + (lane >> 3); int gr = m0 + row; if (gr > S - 1) gr = S - 1;
        va[p] = gr * (K * 2) + (((lane & 7) ^ ((row >> 1) & 7)) << 4);
    }
    vb = (w * 16 + (lane >> 5) * 4 + ((lane >> 2) & 3)) * (D * 2) + (n0 + ((lane >> 4) & 1) * 32 + (lane & 3) * 8) * 2;
    auto issue = [&](int t, int sl) {
        const unsigned slot = lds0 + sl * RS_STAGE;
        const char* ab = A + t * 128;
        const char* wb = W + (size_t)t * 64 * (D * 2);
#pragma unroll
        for (int p = 0; p < 3; ++p) dma16(ab, va[p], slot + (w * 3 + p) * 1024);
#pragma unroll
        for (int p = 0; p < 2; ++p) dma16(wb + p * 8 * (D * 2), vb, slot + 12288 + (w * 2 + p) * 1024);
    };
    unsigned aoff[2], boff;
#pragma unroll
    for (int ks = 0; ks < 2; ++ks) aoff[ks] = l31 * 128 + (((2 * (2 * kh + ks) + hh) ^ ((lane >> 1) & 7)) << 4);
    boff = 12288 + (8 * kh + 2 * hh) * 512 + nh * 256 + ((lane & 15) >> 2) * 64 + ((lane >> 4) & 1) * 32 + (lane & 3) * 8;
    f32x16 acc[3];
#pragma unroll
    for (int i = 0; i < 3; ++i) acc[i] = f32x16{};
#pragma unroll
    for (int t = 0; t < RS_NST - 1; ++t) issue(t, t);
    int sl = 0, slf = RS_NST - 1;
    auto compute = [&](int s) {
        lds_ptr sb = lds + s * RS_STAGE;
#pragma unroll
        for (int ks = 0; ks < 2; ++ks) {
            u32x2 lo = ldstr(sb + boff + ks * 2048), hi = ldstr(sb + boff + ks * 2048 + 512);
            const u32x4 wf = {lo[0], lo[1], hi[0], hi[1]};
#pragma unroll
            for (int i = 0; i < 3; ++i) acc[i] = mfma32(wf, lds128(sb + aoff[ks] + i * 4096), acc[i]);
        }
    };
    for (int t = 0; t < NT - (RS_NST - 1); ++t) {
        wait_bar<5 * (RS_NST - 2)>();
        issue(t + RS_NST - 1, slf);
        compute(sl);
        slf = sl; sl = sl + 1 == RS_NST ? 0 : sl + 1;
    }
#pragma unroll
    for (int t = 0; t < RS_NST - 1; ++t) {
        if (t == 0) wait_bar<20>(); else if (t == 1) wait_bar<15>(); else if (t == 2) wait_bar<10>(); else if (t == 3) wait_bar<5>(); else wait_bar<0>();
        compute(sl);
        sl = sl + 1 == RS_NST ? 0 : sl + 1;
    }
    STAMP(ws_base, BIAS ? 4 : 2, 1);
    static_assert(RS_NST == 6, "tail waits are written for six slots");
    lds_bar();
    constexpr int RP = 272;
#pragma unroll
    for (int i = 0; i < 3; ++i)
#pragma unroll
        for (int g = 0; g < 4; ++g)
            *(LAS f32x4*)(lds + (kh * 96 + i * 32 + l31) * RP + (nh * 32 + g * 8 + hh * 4) * 4) =
                (f32x4){acc[i][4 * g], acc[i][4 * g + 1], acc[i][4 * g + 2], acc[i][4 * g + 3]};
    lds_bar();
#pragma unroll
    for (int r = 0; r < 3; ++r) {
        const int it = tid + 256 * r, row = it >> 3, c8 = it & 7;
        lds_ptr p = lds + row * RP + c8 * 32;
        const f32x4 a0 = *(LAS const f32x4*)p, a1 = *(LAS const f32x4*)(p + 16);
        const f32x4 b0 = *(LAS const f32x4*)(p + 96 * RP), b1 = *(LAS const f32x4*)(p + 96 * RP + 16);
        float v[8];
#pragma unroll
        for (int e = 0; e < 4; ++e) { v[e] = a0[e] + b0[e]; v[4 + e] = a1[e] + b1[e]; }
#pragma unroll
        for (int e = 0; e < 4; ++e) {
            v[2 * e] += bflo(rv[r][e]); v[2 * e + 1] += bfhi(rv[r][e]);
            if (BIAS) { v[2 * e] += bflo(bv[e]); v[2 * e + 1] += bfhi(bv[e]); }
        }
        if (m0 + row < S)
            *(u32x4*)(out + (size_t)(m0 + row) * (D * 2) + (n0 + c8 * 8) * 2) =
                (u32x4){pk2(v[0], v[1]), pk2(v[2], v[3]), pk2(v[4], v[5]), pk2(v[6], v[7])};
    }
    STAMP(ws_base, BIAS ? 4 : 2, 3);
}

constexpr int AT_NSL = 4;
#define SBAR() __builtin_amdgcn_sched_barrier(0)
__device__ __forceinline__ f32x16 smfma(u32x4 a, f32x8 b, f32x16 c, unsigned idx) {
    return __builtin_amdgcn_smfmac_f32_32x32x32_bf16(__builtin_bit_cast(bf16x8, a), __builtin_bit_cast(bf16x16, b), c, idx, 0, 0);
}
__device__ __forceinline__ f32x8 lo8(const f32x16& s) { return __builtin_shufflevector(s, s, 0, 1, 2, 3, 4, 5, 6, 7); }
__device__ __forceinline__ f32x8 hi8(const f32x16& s) { return __builtin_shufflevector(s, s, 8, 9, 10, 11, 12, 13, 14, 15); }

__global__ __launch_bounds__(512, 2) void k_attn(char* __restrict__ ws_base) {
    extern __shared__ __attribute__((aligned(16))) char smem[];
    const int tid = threadIdx.x, lane = tid & 63, w = __builtin_amdgcn_readfirstlane(tid >> 6);
    const int hh = lane >> 5, l31 = lane & 31;
    const int x = blockIdx.x & 7, jx = blockIdx.x >> 3;
    STAMP(ws_base, 1, 0);
    const int head = x * 4 + (jx >> 3), qb = jx & 7;
    const char* kv = ws_base + WS_KV + (size_t)head * 8 * CHUNK;
    const char* qp = ws_base + WS_Q + (size_t)(head * 8 + qb) * QCH;
    lds_ptr lds = (lds_ptr)smem;
    const unsigned lds0 = (unsigned)(uintptr_t)smem;
    const int p2 = w + 16 > 20 ? 20 : w + 16;
    const unsigned vlane = lane * 16;
    auto issue = [&](int c) {
        const unsigned slot = lds0 + (c & (AT_NSL - 1)) * CHUNK;
        const char* src = kv + c * CHUNK;
        dma16(src + w * 1024, vlane, slot + w * 1024);
        dma16(src + (w + 8) * 1024, vlane, slot + (w + 8) * 1024);
        dma16(src + p2 * 1024, vlane, slot + p2 * 1024);
    };
    const u32x4 qm = *(const u32x4*)(qp + w * 1024 + lane * 16);
    const int ql = w * 32 + l31;
    const u32x2 qr = *(const u32x2*)(qp + 8192 + (ql >> 7) * 1024 + (ql & 63) * 16 + ((ql >> 6) & 1) * 8);
    const u32x2 qe = hh == 0 ? qr : (u32x2){0u, 0u}, qo = hh == 1 ? qr : (u32x2){0u, 0u};
    const unsigned koff = lane * 16;
    const unsigned voff = 10240 + (l31 < 20 ? (hh * 20 + l31) * 16 : (l31 == 20 ? 640 : 656));
    unsigned idx = 0xddddddddu;
    asm volatile("" : "+v"(idx));
    f32x16 o;
    float mrow = 0.f;
    for (int attempt = 0; attempt < 2; ++attempt) {
        issue(0); issue(1); issue(2);
        wait_bar<3>();
        STAMP(ws_base, 1, 1);
        u32x4 km, kr, vl, vh;
        f32x16 sa, sb, nm;
        km = lds128(lds + koff); kr = lds128(lds + 8192 + koff);
        if (attempt == 0) {
            sa = mfma32(km, qm, f32x16{});
            sa = mfma8((u32x2){kr[0], kr[1]}, qe, sa);
            float m = sa[0];
#pragma unroll
            for (int r = 1; r < 16; ++r) m = fmaxf(m, sa[r]);
            mrow = fmaxf(m, xhalf(m));
        }
#pragma unroll
        for (int r = 0; r < 16; ++r) nm[r] = -mrow;
        asm volatile("" : "+v"(nm));
        sa = mfma32(km, qm, nm);
        sa = mfma8((u32x2){kr[0], kr[1]}, qe, sa);
        km = lds128(lds + 1024 + koff);
        vl = lds128(lds + voff);
        vh = (u32x4){0u, 0u, 0u, 0u};
        sb = f32x16{};
        o = f32x16{};
#define EXP4(X, i) do { X[i] = __builtin_amdgcn_exp2f(X[i]); X[i + 1] = __builtin_amdgcn_exp2f(X[i + 1]); \
                        X[i + 2] = __builtin_amdgcn_exp2f(X[i + 2]); X[i + 3] = __builtin_amdgcn_exp2f(X[i + 3]); asm volatile("" : "+v"(X)); } while (0)
#define TILE(T, CUR, NXT) do { \
        o = smfma(vh, hi8(NXT), o, idx); SBAR(); \
        vh = lds128(cur + voff + (T) * 1408 + 704); \
        EXP4(CUR, 0); SBAR(); \
        NXT = mfma32(km, qm, nm); SBAR(); \
        km = lds128(((T) + 2 < 8 ? cur : nxt) + koff + (((T) + 2) & 7) * 1024); \
        EXP4(CUR, 4); SBAR(); \
        NXT = mfma8((((T) + 1) & 2) ? (u32x2){kr[2], kr[3]} : (u32x2){kr[0], kr[1]}, (((T) + 1) & 1) ? qo : qe, NXT); SBAR(); \
        if ((((T) + 1) & 3) == 3) kr = lds128(((T) + 2 < 8 ? cur : nxt) + 8192 + koff + ((((T) + 2) >> 2) & 1) * 1024); \
        EXP4(CUR, 8); SBAR(); \
        o = smfma(vl, lo8(CUR), o, idx); SBAR(); \
        vl = lds128(((T) + 1 < 8 ? cur : nxt) + voff + (((T) + 1) & 7) * 1408); \
        EXP4(CUR, 12); SBAR(); \
        } while (0)
        for (int c = 0; c < 8; ++c) {
            if (c > 0) { if (c < 6) wait_bar<3>(); else wait_bar<0>(); }
            if (c + 3 < 8) issue(c + 3);
            lds_ptr cur = lds + (c & (AT_NSL - 1)) * CHUNK, nxt = lds + ((c + 1) & (AT_NSL - 1)) * CHUNK;
            TILE(0, sa, sb); TILE(1, sb, sa); TILE(2, sa, sb); TILE(3, sb, sa);
            TILE(4, sa, sb); TILE(5, sb, sa); TILE(6, sa, sb); TILE(7, sb, sa);
        }
        o = smfma(vh, hi8(sb), o, idx);
        STAMP(ws_base, 1, 2);
        float chk = 0.f;
#pragma unroll
        for (int r = 0; r < 12; ++r) chk += fabsf(o[r]);
        const bool bad = !(chk < 1e30f);
        if (attempt == 1 || !__syncthreads_or(bad)) break;
        float m = -INFINITY;
        for (int t = 0; t < 64; ++t) {
            const char* cb = kv + (t >> 3) * CHUNK;
            const u32x4 a = *(const u32x4*)(cb + (t & 7) * 1024 + lane * 16);
            const u32x2 b = *(const u32x2*)(cb + 8192 + ((t >> 2) & 1) * 1024 + lane * 16 + ((t >> 1) & 1) * 8);
            f32x16 s = mfma32(a, qm, f32x16{});
            s = mfma8(b, (t & 1) ? qo : qe, s);
#pragma unroll
            for (int r = 0; r < 16; ++r) m = fmaxf(m, s[r]);
        }
        mrow = fmaxf(m, xhalf(m));
    }
    const float lx = xhalf(o[8]);
    const float inv = 1.0f / (hh ? o[8] : lx);
    char* orow = ws_base + WS_O + (size_t)(qb * 256 + ql) * (D * 2) + head * 40 + hh * 8;
#pragma unroll
    for (int g = 0; g < 3; ++g)
        if (g < 2 || hh == 0)
            *(u32x2*)(orow + g * 16) = (u32x2){pk2(o[4 * g] * inv, o[4 * g + 1] * inv), pk2(o[4 * g + 2] * inv, o[4 * g + 3] * inv)};
    STAMP(ws_base, 1, 3);
}
}

extern "C" void kernel_launch(void* const* d_in, const int* in_sizes, int n_in, void* d_out, int out_size, void* d_ws, size_t ws_size,
                              hipStream_t stream) {
    const char* x = (const char*)d_in[0];
    const char* wqkv = (const char*)d_in[1];
    const char* wo = (const char*)d_in[2];
    const char* w1 = (const char*)d_in[4];
    const uint16_t* b1 = (const uint16_t*)d_in[5];
    const char* w2 = (const char*)d_in[6];
    const uint16_t* b2 = (const uint16_t*)d_in[7];
    char* ws = (char*)d_ws;
    (void)hipFuncSetAttribute((const void*)k_gemm_ln<0>, hipFuncAttributeMaxDynamicSharedMemorySize, LN_NST * LN_STAGE);
    (void)hipFuncSetAttribute((const void*)k_gemm_ln<1>, hipFuncAttributeMaxDynamicSharedMemorySize, LN_NST * LN_STAGE);
    (void)hipFuncSetAttribute((const void*)k_gemm_res<640, false>, hipFuncAttributeMaxDynamicSharedMemorySize, RS_NST * RS_STAGE);
    (void)hipFuncSetAttribute((const void*)k_gemm_res<FF, true>, hipFuncAttributeMaxDynamicSharedMemorySize, RS_NST * RS_STAGE);
    (void)hipFuncSetAttribute((const void*)k_attn, hipFuncAttributeMaxDynamicSharedMemorySize, AT_NSL * CHUNK);
    k_gemm_ln<0><<<240, 256, LN_NST * LN_STAGE, stream>>>(x, wqkv, nullptr, ws);
    k_attn<<<256, 512, AT_NSL * CHUNK, stream>>>(ws);
    k_gemm_res<640, false><<<220, 256, RS_NST * RS_STAGE, stream>>>(ws + WS_O, wo, x, nullptr, ws + WS_H, ws);
    k_gemm_ln<1><<<224, 256, LN_NST * LN_STAGE, stream>>>(ws + WS_H, w1, b1, ws);
    k_gemm_res<FF, true><<<220, 256, RS_NST * RS_STAGE, stream>>>(ws + WS_H1, w2, ws + WS_H, b2, (char*)d_out, ws);
}
```

```cpp
#include <hip/hip_runtime.h>
#include <stdint.h>

#define ROT1 0
namespace {
constexpr int S = 2048, D = 640, FF = 1728;
typedef __attribute__((ext_vector_type(16))) float f32x16;
typedef __attribute__((ext_vector_type(8))) float f32x8;
typedef __attribute__((ext_vector_type(4))) float f32x4;
typedef __attribute__((ext_vector_type(2))) float f32x2;
typedef __attribute__((ext_vector_type(8))) __bf16 bf16x8;
typedef __attribute__((ext_vector_type(16))) __bf16 bf16x16;
typedef __attribute__((ext_vector_type(2))) __bf16 bf16x2;
typedef __attribute__((ext_vector_type(4))) short s16x4;
typedef __attribute__((ext_vector_type(4))) unsigned u32x4;
typedef __attribute__((ext_vector_type(2))) unsigned u32x2;
typedef unsigned long long u64;
typedef __amdgpu_buffer_rsrc_t rsrc_t;
#define LAS __attribute__((address_space(3)))
#define GAS __attribute__((address_space(1)))
typedef LAS char* lds_ptr;

constexpr int W_NONCE = 0;
constexpr int FL = 128;
constexpr int W_F0 = 1024;
constexpr int W_F1 = W_F0 + 256 * FL;
constexpr int W_F2 = W_F1 + 256 * FL;
constexpr int W_F3 = W_F2 + 256 * FL;
constexpr int W_F3S = W_F3 + 256 * FL;
constexpr int W_FR = W_F3S + 256 * FL;
constexpr int W_FW = W_FR + 256 * FL;
constexpr int W_F4 = W_FW + 16 * FL;
constexpr int W_RS = W_F4 + 2048 * FL;
constexpr int W_CSP = W_RS + S * 8;
constexpr int W_CS1 = W_CSP + 16 * 1920 * 4;
constexpr int W_HST = W_CS1 + 16 * FF * 4 + 256;
constexpr int W_KM = 1 << 20;
constexpr int W_QM = W_KM + 32 * 65536;
constexpr int W_KR = W_QM + 32 * 65536;
constexpr int W_QR = W_KR + 16 * 32768;
constexpr int W_VT = W_QR + 16 * 32768;
constexpr int W_O = W_VT + 32 * 90112 + 1024;
constexpr int W_H = W_O + 32 * 81920;
constexpr int W_H1 = W_H + 32 * 81920;
constexpr int W_WOT = W_H1 + 32 * 221184;
constexpr int W_W2T = W_WOT + 8 * 102400;
constexpr int W_W1T = W_W2T + 8 * 276480;
constexpr int W_END = W_W1T + 54 * 40960;
static_assert(W_HST + 32 * 8 * 512 <= W_KM, "control area");

constexpr int LDS_BYTES = 163840;
constexpr int L_P1SCR = 131072;
constexpr int L_TAG = L_P1SCR + 28672;
constexpr int AT_SLOT = 24576;
constexpr int L_B80 = 73728;
constexpr int L_PERS = 81920;
constexpr int P5_SLOT = 40960;
constexpr int L_CB = 2 * P5_SLOT + 36864;
constexpr int L_HS = 3 * P5_SLOT + 36864;
constexpr int L_CNT = L_HS + 1024;
constexpr int CODE_BYTES = 24576;

#define STAMP(idx)

#define DMA_I(v, o, c) "buffer_load_dwordx4 " v ", %1, %2 offen offset:" #o c " lds\n\t"
#define DMA_HEAD "s_mov_b32 m0, %0\n\ts_nop 3\n\t"
#define DMA_OPS(l, r, s) "s"(__builtin_amdgcn_readfirstlane(l)), "s"(r), "s"(__builtin_amdgcn_readfirstlane(s))
__device__ __forceinline__ void dma1(rsrc_t r, unsigned so, unsigned l, unsigned a) { asm volatile(DMA_HEAD DMA_I("%3", 0, "") :: DMA_OPS(l, r, so), "v"(a) : "memory"); }
__device__ __forceinline__ void dma4(rsrc_t r, unsigned so, unsigned l, unsigned a, unsigned b, unsigned c, unsigned d) {
    asm volatile(DMA_HEAD DMA_I("%3", 0, "") DMA_I("%4", 1024, "") DMA_I("%5", 2048, "") DMA_I("%6", 3072, "") :: DMA_OPS(l, r, so), "v"(a), "v"(b), "v"(c), "v"(d) : "memory"); }
template <int N> __device__ __forceinline__ void dmal(rsrc_t r, unsigned so, unsigned l, unsigned v) {
    if (N == 1) asm volatile(DMA_HEAD DMA_I("%3", 0, " sc1") :: DMA_OPS(l, r, so), "v"(v) : "memory");
    if (N == 2) asm volatile(DMA_HEAD DMA_I("%3", 0, " sc1") DMA_I("%3", 1024, " sc1") :: DMA_OPS(l, r, so), "v"(v) : "memory");
    if (N == 3) asm volatile(DMA_HEAD DMA_I("%3", 0, " sc1") DMA_I("%3", 1024, " sc1") DMA_I("%3", 2048, " sc1") :: DMA_OPS(l, r, so), "v"(v) : "memory");
    if (N == 4) asm volatile(DMA_HEAD DMA_I("%3", 0, " sc1") DMA_I("%3", 1024, " sc1") DMA_I("%3", 2048, " sc1") DMA_I("%3", 3072, " sc1") :: DMA_OPS(l, r, so), "v"(v) : "memory");
}
__device__ __forceinline__ void touch(rsrc_t r, unsigned so, unsigned l, unsigned v) {
    asm volatile(DMA_HEAD "buffer_load_dword %3, %1, %2 offen sc1 lds" :: DMA_OPS(l, r, so), "v"(v) : "memory"); }
#define BAR() asm volatile("s_waitcnt lgkmcnt(0)\n\ts_barrier" ::: "memory")
#define BARN() asm volatile("s_barrier" ::: "memory")
#define WB(n) asm volatile("s_waitcnt vmcnt(" #n ") lgkmcnt(0)\n\ts_barrier" ::: "memory")
#define TR_ARGS
#define TRC()
#define TR_PASS
#define TR_SET(ph)
#define DRAIN() asm volatile("s_waitcnt vmcnt(0)" ::: "memory")
__device__ __forceinline__ u32x4 lds128(lds_ptr p) { return *(LAS const u32x4*)p; }
__device__ __forceinline__ u32x2 lds64(lds_ptr p) { return *(LAS const u32x2*)p; }
__device__ __forceinline__ u32x2 ldstr(lds_ptr p) { return __builtin_bit_cast(u32x2, __builtin_amdgcn_ds_read_tr16_b64_v4i16((LAS s16x4*)p)); }
__device__ __forceinline__ u32x4 cat(u32x2 a, u32x2 b) { return (u32x4){a[0], a[1], b[0], b[1]}; }
__device__ __forceinline__ f32x16 mfma32(u32x4 a, u32x4 b, f32x16 c) {
    return __builtin_amdgcn_mfma_f32_32x32x16_bf16(__builtin_bit_cast(bf16x8, a), __builtin_bit_cast(bf16x8, b), c, 0, 0, 0);
}
__device__ __forceinline__ f32x4 mfma16(u32x4 a, u32x4 b, f32x4 c) {
    return __builtin_amdgcn_mfma_f32_16x16x32_bf16(__builtin_bit_cast(bf16x8, a), __builtin_bit_cast(bf16x8, b), c, 0, 0, 0);
}
__device__ __forceinline__ f32x16 mfma8(u32x2 a, u32x2 b, f32x16 c) {
    return __builtin_amdgcn_mfma_f32_32x32x8bf16_1k(__builtin_bit_cast(s16x4, a), __builtin_bit_cast(s16x4, b), c, 0, 0, 0);
}
__device__ __forceinline__ f32x16 smfma(u32x4 a, f32x8 b, f32x16 c, unsigned idx) {
    return __builtin_amdgcn_smfmac_f32_32x32x32_bf16(__builtin_bit_cast(bf16x8, a), __builtin_bit_cast(bf16x16, b), c, idx, 0, 0);
}
__device__ __forceinline__ f32x8 lo8(const f32x16& s) { return __builtin_shufflevector(s, s, 0, 1, 2, 3, 4, 5, 6, 7); }
__device__ __forceinline__ f32x8 hi8(const f32x16& s) { return __builtin_shufflevector(s, s, 8, 9, 10, 11, 12, 13, 14, 15); }
__device__ __forceinline__ unsigned pk2(float lo, float hi) { bf16x2 v = {(__bf16)lo, (__bf16)hi}; return __builtin_bit_cast(unsigned, v); }
__device__ __forceinline__ float bflo(unsigned u) { return __uint_as_float(u << 16); }
__device__ __forceinline__ float bfhi(unsigned u) { return __uint_as_float(u & 0xffff0000u); }
__device__ __forceinline__ float fma1(float a, float b, float c) { float d; asm("v_fma_f32 %0, %1, %2, %3" : "=v"(d) : "v"(a), "v"(b), "v"(c)); return d; }
__device__ __forceinline__ float add1(float a, float b) { float d; asm("v_add_f32 %0, %1, %2" : "=v"(d) : "v"(a), "v"(b)); return d; }
__device__ __forceinline__ float mul1(float a, float b) { float d; asm("v_mul_f32 %0, %1, %2" : "=v"(d) : "v"(a), "v"(b)); return d; }
__device__ __forceinline__ float half_sum(float v) {
    auto r = __builtin_amdgcn_permlane32_swap(__float_as_uint(v), __float_as_uint(v), false, false);
    return __uint_as_float(r[0]) + __uint_as_float(r[1]);
}
__device__ __forceinline__ float half_max(float v) {
    auto r = __builtin_amdgcn_permlane32_swap(__float_as_uint(v), __float_as_uint(v), false, false);
    return fmaxf(__uint_as_float(r[0]), __uint_as_float(r[1]));
}
template <int C> __device__ __forceinline__ float dpp(float v) { return __builtin_bit_cast(float, __builtin_amdgcn_mov_dpp(__builtin_bit_cast(int, v), C, 0xf, 0xf, true)); }
__device__ __forceinline__ float wave_sum(float v) {
    v += dpp<0xB1>(v); v += dpp<0x4E>(v); v += dpp<0x141>(v); v += dpp<0x128>(v);
    auto r = __builtin_amdgcn_permlane16_swap(__float_as_uint(v), __float_as_uint(v), false, false);
    return half_sum(__uint_as_float(r[0]) + __uint_as_float(r[1]));
}
__device__ __forceinline__ u32x4 widen(u32x2 a, u32x2 b) {
    auto r0 = __builtin_amdgcn_permlane32_swap(a[0], b[0], false, false);
    auto r1 = __builtin_amdgcn_permlane32_swap(a[1], b[1], false, false);
    return (u32x4){r0[0], r1[0], r0[1], r1[1]};
}
constexpr int SC1 = 16;
__device__ __forceinline__ void st128(rsrc_t r, int off, u32x4 v) { __builtin_amdgcn_raw_buffer_store_b128(v, r, off, 0, SC1); }
__device__ __forceinline__ void st32(rsrc_t r, int off, float v) { __builtin_amdgcn_raw_buffer_store_b32(__float_as_uint(v), r, off, 0, SC1); }
__device__ __forceinline__ void st64(rsrc_t r, int off, u32x2 v) { __builtin_amdgcn_raw_buffer_store_b64(v, r, off, 0, SC1); }
__device__ __forceinline__ u32x4 ld128(rsrc_t r, int off) { return __builtin_amdgcn_raw_buffer_load_b128(r, off, 0, SC1); }
__device__ __forceinline__ u32x2 ld64(rsrc_t r, int off) { return __builtin_amdgcn_raw_buffer_load_b64(r, off, 0, SC1); }
__device__ __forceinline__ float ld32f(rsrc_t r, int off) { return __uint_as_float(__builtin_amdgcn_raw_buffer_load_b32(r, off, 0, SC1)); }
__device__ __forceinline__ void set_flag(char* ws, int off, u64 tag) {
    __hip_atomic_store((GAS u64*)(ws + off), tag, __ATOMIC_RELAXED, __HIP_MEMORY_SCOPE_AGENT);
}
__device__ __forceinline__ void wait_flags(char* ws, int off, u64 tag) {
#pragma unroll 1
    for (int it = 0; it < 40000; ++it) {
        const u64 v = __hip_atomic_load((GAS u64*)(ws + off), __ATOMIC_RELAXED, __HIP_MEMORY_SCOPE_AGENT);
        if (__all(v == tag)) break;
        __builtin_amdgcn_s_sleep(1);
    }
    __builtin_amdgcn_fence(__ATOMIC_ACQUIRE, "wavefront");
}
__device__ __forceinline__ u64 ld_flag(char* ws, int off) { return __hip_atomic_load((GAS u64*)(ws + off), __ATOMIC_RELAXED, __HIP_MEMORY_SCOPE_AGENT); }
__device__ __forceinline__ u64 uni64(u64 v) {
    return ((u64)(unsigned)__builtin_amdgcn_readfirstlane((unsigned)(v >> 32)) << 32) | (unsigned)__builtin_amdgcn_readfirstlane((unsigned)v);
}
#define SBAR() __builtin_amdgcn_sched_barrier(0)
#define PIN(x) asm volatile("" : "+v"(x))

__device__ __forceinline__ f32x2 row_stats(const char* x, int row, int lane) {
    u32x4 a = {}, b = {};
    if (lane < 40) { const char* p = x + row * (D * 2) + lane * 32; a = *(const u32x4*)p; b = *(const u32x4*)(p + 16); }
    float v[16];
#pragma unroll
    for (int e = 0; e < 4; ++e) { v[2 * e] = bflo(a[e]); v[2 * e + 1] = bfhi(a[e]); v[8 + 2 * e] = bflo(b[e]); v[9 + 2 * e] = bfhi(b[e]); }
    float s = 0.f;
#pragma unroll
    for (int e = 0; e < 16; ++e) s += v[e];
    const float mean = wave_sum(s) * (1.0f / D);
    float q = 0.f;
#pragma unroll
    for (int e = 0; e < 16; ++e) { const float d = v[e] - mean; q += d * d; }
    q = lane < 40 ? q : 0.f;
    return (f32x2){mean, __builtin_amdgcn_rsqf(wave_sum(q) * (1.0f / D) + 1e-5f)};
}

template <bool CT, class Hook>
__device__ __forceinline__ void loop32(lds_ptr lds, const unsigned (&aoff)[4], unsigned boff, f32x16 (&acc)[2], Hook hook TR_ARGS) {
    constexpr int ASTG = 32768, BSTG = 32768, astep = 4096;
    u32x4 xa[4][2], wf[4];
    auto mm = [&](int k, int i) { acc[i] = CT ? mfma32(wf[k], xa[k][i], acc[i]) : mfma32(xa[k][i], wf[k], acc[i]); SBAR(); };
    auto rdb = [&](lds_ptr bp, int k) { return cat(ldstr(bp + k * 4096), ldstr(bp + k * 4096 + 1024)); };
    auto half = [&](int m, int r, int sl) {
        lds_ptr a0 = lds + sl * ASTG + aoff[r], a1 = lds + sl * ASTG + aoff[r + 1], bp = lds + sl * BSTG + boff;
        mm(m, 0);
        wf[r] = rdb(bp, r); xa[r][0] = lds128(a0); SBAR();
        mm(m, 1);
        xa[r][1] = lds128(a0 + astep); wf[r + 1] = rdb(bp, r + 1); SBAR();
        mm(m + 1, 0);
        xa[r + 1][0] = lds128(a1); xa[r + 1][1] = lds128(a1 + astep); SBAR();
        mm(m + 1, 1);
    };
#pragma unroll
    for (int k = 0; k < 2; ++k) { wf[k] = rdb(lds + boff, k); xa[k][0] = lds128(lds + aoff[k]); xa[k][1] = lds128(lds + aoff[k] + astep); }
#pragma unroll 1
    for (int t = 0; t < 10; ++t) {
        half(0, 2, t & 3);
        hook(t);
        BAR();
        half(2, 0, (t + 1) & 3);
    }
}

__device__ __forceinline__ int kpos(int g, int j) { return j < 4 ? g * 4 + j : 28 + (g < 6 ? 3 * g : 6 + 2 * g) + j; }

__global__ __launch_bounds__(768, 3) void k_block(const char* __restrict__ x, const char* __restrict__ wqkv, char* __restrict__ ws,
                                                  const char* __restrict__ w1, const char* __restrict__ wo, const char* __restrict__ w2,
                                                  const char* __restrict__ b1, const char* __restrict__ b2, char* __restrict__ out) {
    extern __shared__ __attribute__((aligned(16))) char smem[];
    lds_ptr lds = (lds_ptr)smem;
    const unsigned lds0 = (unsigned)(uintptr_t)smem;
    const char* pc;
    asm volatile("s_getpc_b64 %0" : "=s"(pc));
    const int tid = threadIdx.x, lane = tid & 63, w = __builtin_amdgcn_readfirstlane(tid >> 6);
    const int hh = lane >> 5, l31 = lane & 31;
    const int xcd = blockIdx.x & 7, jx = blockIdx.x >> 3;
    const bool gemm = jx < 30;
    const int rem = (xcd & 1) * 30 + jx;
    const int mt = gemm ? (xcd >> 1) * 4 + (rem & 3) : xcd * 2 + jx - 30, nt = gemm ? rem >> 2 : 15;
    const int m0 = mt * 128, n0 = nt * 128;
    const rsrc_t rws = __builtin_amdgcn_make_buffer_rsrc(ws, 0, W_END, 0x00020000);
    const int head = xcd * 4 + (jx >> 3), qb = jx & 7;
    const int rb = head, cg = qb;
    const int t4 = cg < 6 ? cg * 7 : 6 + cg * 6;
    const unsigned vlin = lane * 16;
    STAMP(0);
#define TR_DUMP()

    if (w >= 8) {
        const int lw = w - 8;
        if (gemm) {
            const rsrc_t rx = __builtin_amdgcn_make_buffer_rsrc((void*)x, 0, S * D * 2, 0x00020000);
            const rsrc_t rw = __builtin_amdgcn_make_buffer_rsrc((void*)wqkv, 0, D * 3 * D * 2, 0x00020000);
            unsigned va[4], vb[4];
#pragma unroll
            for (int p = 0; p < 4; ++p) {
                const int row = p * 8 + (lane >> 3);
                va[p] = row * (D * 2) + (((lane & 7) ^ ((row >> 1) & 7)) << 4) - p * 1024;
                vb[p] = (p * 4 + ((lane >> 2) & 3)) * (3 * D * 2) + (lane >> 4) * 64 + (lane & 3) * 16 - p * 1024;
            }
            const int sa = (m0 + lw * 32) * (D * 2), sb = lw * 16 * (3 * D * 2) + n0 * 2;
            int kt = ROT1;
            auto issue = [&](int t) {
                const unsigned slot = lds0 + (t & 3) * 32768 + lw * 4096;
                dma4(rx, sa + kt * 128, slot, va[0], va[1], va[2], va[3]);
                dma4(rw, sb + kt * 64 * (3 * D * 2), slot + 16384, vb[0], vb[1], vb[2], vb[3]);
                kt = kt == 9 ? 0 : kt + 1;
            };
#pragma unroll 1
            for (int t = 0; t < 4; ++t) { TRC(); issue(t); if (t == 1) { STAMP(10); WB(8); STAMP(11); } }
#pragma unroll 1
            for (int t = 1; t <= 4; ++t) { WB(16); issue(t + 3); }
            WB(16);
            const unsigned fl = L_P1SCR + 16384 + lw * 1024;
            const int f0 = W_F0 + ((lane & 16) ? (lane & 15) * 16 + nt : mt * 16 + (lane & 15)) * FL;
            dmal<1>(rws, 0, lds0 + fl, f0);
            issue(8);
            WB(17);
            issue(9);
            WB(16);
            const u64 tag = *(LAS u64*)(lds + L_TAG);
            if (!__all(*(LAS u64*)(lds + fl + vlin) == tag)) wait_flags(ws, f0, tag);
            dmal<1>(rws, W_RS + m0 * 4, lds0 + L_P1SCR + 12288, hh * 8192 + l31 * 16);
            dmal<1>(rws, W_CSP + (lw * 4 * 1920 + n0) * 4, lds0 + L_P1SCR + 4096 + lw * 2048, hh * 7680 + l31 * 16);
            dmal<1>(rws, W_CSP + ((lw * 4 + 2) * 1920 + n0) * 4, lds0 + L_P1SCR + 4096 + lw * 2048 + 1024, hh * 7680 + l31 * 16);
            WB(11);
            WB(0);
            float c = 0.f;
#pragma unroll
            for (int i = 0; i < 8; ++i) c += *(LAS float*)(lds + L_P1SCR + 4096 + (hh * 8 + i) * 512 + (lw * 32 + l31) * 4);
            *(LAS float*)(lds + L_P1SCR + 13312 + (lw * 32 + l31) * 4) = half_sum(c);
            BAR();
        } else {
            BAR();
        }
        int rt_dst; unsigned rt_rd, rt_step, rt_hi, v[5];
        const char* src;
        {
            const int un = blockIdx.x * 4 + lw;
            const unsigned stg = L_B80 + lw * 5120;
            if (un < 592) {
                const bool two = un >= 160;
                const int u2 = two ? un - 160 : un, ks = u2 >> 3, g = u2 & 7;
                src = (two ? w2 : wo) + ks * 32 * (D * 2) + g * 160;
#pragma unroll
                for (int p = 0; p < 5; ++p) {
                    const int u = p * 64 + lane, pos = (u * 6554) >> 16, c = u - pos * 10;
                    v[p] = ((pos & 0x13) | ((pos & 4) << 1) | ((pos & 8) >> 1)) * (D * 2) + c * 16 - (p & 3) * 1024;
                }
                const int kg = ks < 42 ? ks / 7 : (ks - 6) / 6;
                rt_dst = two ? W_W2T + g * 276480 + kpos(kg, ks - (kg < 6 ? kg * 7 : 6 + kg * 6)) * 5120 : W_WOT + g * 102400 + ks * 5120;
                rt_rd = stg + ((lane >> 5) * 16 + ((lane >> 4) & 1) * 4 + ((lane & 15) >> 2)) * 160 + (lane & 3) * 8; rt_step = 32; rt_hi = 1280;
            } else {
                const int u2 = un - 592;
                src = w1 + (u2 & 7) * 80 * (FF * 2) + (u2 >> 3) * 64;
#pragma unroll
                for (int p = 0; p < 5; ++p) v[p] = (p * 16 + (lane >> 2)) * (FF * 2) + (lane & 3) * 16 - (p & 3) * 1024;
                rt_dst = W_W1T + u2 * 5120;
                rt_rd = stg + hh * 512 + ((lane & 15) >> 2) * 64 + ((lane >> 4) & 1) * 32 + (lane & 3) * 8; rt_step = 1024; rt_hi = 256;
            }
#pragma unroll
            for (int p = 0; p < 5; ++p) PIN(v[p]);
            PIN(rt_rd);
        }
        BAR();
        __builtin_amdgcn_s_setprio(2);
        STAMP(1); TR_SET(2);
        const u64 tag = uni64(*(LAS u64*)(lds + L_TAG));
        if (lw == 0) {
            const int blk = lane >> 4, c0 = head * 20 + (blk & 1) * 19;
            wait_flags(ws, W_F1 + ((5 + (blk >> 1) * 5 + (c0 >> 7)) * 16 + (lane & 15)) * FL, tag);
        } else if (lw == 1) {
            wait_flags(ws, W_F1 + (((head * 20 + (lane & 1) * 19) >> 7) * 16 + qb * 2 + ((lane >> 1) & 1)) * FL, tag);
        }
        BAR();
        STAMP(2);
        auto copy5 = [&](int src5, unsigned dst) {
            dmal<4>(rws, src5 + lw * 5120, dst + lw * 5120, vlin);
            dmal<1>(rws, src5 + lw * 5120 + 4096, dst + lw * 5120 + 4096, vlin);
        };
        const int w2s = W_W2T + cg * 276480;
        u32x4 wreg[20];
        {
            auto issue = [&](int c, int slot) {
                const unsigned dst = lds0 + slot * AT_SLOT;
                dmal<2>(rws, W_KM + head * 65536 + c * 8192 + lw * 2048, dst + lw * 2048, vlin);
                dmal<1>(rws, W_KR + (head >> 1) * 32768 + c * 4096 + lw * 1024, dst + 8192 + lw * 1024, vlin);
                dmal<3>(rws, W_VT + head * 90112 + c * 11264 + lw * 3072, dst + 12288 + lw * 3072, vlin);
            };
#pragma unroll 1
            for (int c = 0; c < 3; ++c) { issue(c, c); if (c == 0) WB(0); }
            {
                const rsrc_t rs = __builtin_amdgcn_make_buffer_rsrc((void*)src, 0, 80 * FF * 2, 0x00020000);
                dma4(rs, 0, lds0 + L_B80 + lw * 5120, v[0], v[1], v[2], v[3]);
                dma1(rs, 0, lds0 + L_B80 + lw * 5120 + 4096, v[4]);
            }
            WB(11);
            DRAIN();
#pragma unroll
            for (int p = 0; p < 5; ++p)
                st128(rws, rt_dst + p * 1024 + lane * 16, cat(ldstr(lds + rt_rd + p * rt_step), ldstr(lds + rt_rd + p * rt_step + rt_hi)));
            WB(0);
            if (tid == 512) set_flag(ws, W_FR + blockIdx.x * FL, tag);
            issue(3, 0);
            WB(0); issue(4, 1);
            wait_flags(ws, W_FR + (lw * 64 + lane) * FL, tag);
            WB(0); issue(5, 2);
#pragma unroll 1
            for (int p = 0; p < 4; ++p) copy5(W_WOT + cg * 102400 + p * 20480, lds0 + L_B80 + p * 20480);
            WB(20); issue(6, 0);
#pragma unroll
            for (int p = 0; p < 20; ++p) wreg[p] = __builtin_amdgcn_raw_buffer_load_b128(rws, vlin + (p % 5) * 1024, w2s + ((p / 5 + cg) & 7) * 20480 + lw * 5120, SC1);
            WB(20); issue(7, 1);
            {
                const unsigned junk = lds0 + L_HS;
                const int q = (rb & 3) * 4 + lw;
#pragma unroll 1
                for (int p = 0; p < 5; ++p) touch(rws, W_W1T + t4 * 40960 + q * 17920 + p * 4096, junk, lane * 64);
#pragma unroll 1
                for (int p = 0; p < 5; ++p) touch(rws, w2s + q * 17280 + p * 4096, junk, lane * 64);
            }
            WB(10);
            __builtin_amdgcn_s_waitcnt(0x0F70);
            BAR();
            BAR();
            BAR();
        }
        __builtin_amdgcn_s_setprio(0);
        STAMP(3); TR_SET(3);
        if (lw == 0) wait_flags(ws, W_F2 + (xcd * 32 + l31) * FL, tag);
        BAR();
        STAMP(4);
        {
            const int src = W_O + rb * 81920 + lw * 4096;
#pragma unroll 1
            for (int p = 0; p < 4; ++p) dmal<4>(rws, src + p * 16384, lds0 + p * 16384 + lw * 4096, vlin);
            WB(12);
            WB(8);
            copy5(W_WOT + cg * 102400 + 4 * 20480, lds0 + L_B80); dmal<4>(rws, src + 4 * 16384, lds0 + lw * 4096, vlin);
            WB(13);
            WB(9);
            WB(0);
            BAR();
        }
        STAMP(5);
        auto dump = [&](int p) {
#pragma unroll
            for (int i = 0; i < 5; ++i) *(LAS u32x4*)(lds + ((p + 2) & 3) * P5_SLOT + 16384 + lw * 5120 + i * 1024 + vlin) = wreg[p * 5 + i];
        };
        BAR();
        dump(0); dump(1);
        BAR();
        TR_SET(4);
        if (lw == 0) wait_flags(ws, W_F3 + (rb * 8 + (lane & 7)) * FL, tag);
        BAR();
        STAMP(6);
#pragma unroll 1
        for (int p = 0; p < 5; ++p) dmal<4>(rws, W_H + rb * 81920 + p * 16384 + lw * 4096, lds0 + p * 16384 + lw * 4096, vlin);
        WB(16);
        WB(8);
        WB(0);
        STAMP(7);
        {
            const int src = W_H1 + rb * 221184 + lw * 4096;
            auto issueA = [&](int p) { dmal<4>(rws, src + (p < 8 ? (p + cg) & 7 : p) * 16384, lds0 + ((p + 2) & 3) * P5_SLOT + lw * 4096, vlin); };
            auto issue = [&](int p) { copy5(w2s + (p < 8 ? (p + cg) & 7 : p) * 20480, lds0 + ((p + 2) & 3) * P5_SLOT + 16384); issueA(p); };
            const unsigned nact = cg < 6 ? 7 : 6;
#pragma unroll 1
            for (int it = 0; it < 100000; ++it) {
                if (__hip_atomic_load((LAS unsigned*)(lds + L_CNT), __ATOMIC_RELAXED, __HIP_MEMORY_SCOPE_WORKGROUP) >= nact) break;
                __builtin_amdgcn_s_sleep(2);
            }
            asm volatile("" ::: "memory");
            dump(2); dump(3);
            wait_flags(ws, W_F4 + (rb * 64 + (lane & 7) * 4 + lw) * FL, tag);
            STAMP(8);
#pragma unroll 1
            for (int p = 0; p < 4; ++p) issueA(p);
            TR_SET(5);
            WB(12);
            WB(8); issue(4);
            WB(13); issue(5);
            WB(18); issue(6);
            WB(18);
            const unsigned fl = L_CB + 2048 + lw * 256;
            const int fb = W_F4 + (rb * 64 + 32 + lw) * FL, nb = lw < 2 ? 5 : 4;
            touch(rws, fb, lds0 + fl, min(lane >> 1, nb) * 4 * FL + (lane & 1) * 4);
            issue(7);
            WB(9);
            if (!__all(*(LAS u64*)(lds + fl + l31 * 8) == tag)) wait_flags(ws, fb + min(lane, nb) * 4 * FL, tag);
            issue(8);
#pragma unroll 1
            for (int p = 6; p <= 10; ++p) { WB(18); issue(p + 3); }
            WB(18);
            WB(9);
            WB(0);
            BAR();
            BAR();
        }
        STAMP(9); TR_DUMP();
        return;
    }

    u64 nonce = 0;
    if (w == 0) nonce = ld_flag(ws, W_NONCE);
    if (w == 6) {
        const rsrc_t rc = __builtin_amdgcn_make_buffer_rsrc((void*)pc, 0, CODE_BYTES, 0x00020000);
        touch(rc, (jx & 7) * 4096, lds0 + L_P1SCR + 30720, lane * 64);
    }
    if (gemm) {
        const int wm = w >> 2, wn = w & 3;
        {
            unsigned cv[5];
#pragma unroll
            for (int r = 0; r < 5; ++r) cv[r] = *(const unsigned*)(wqkv + (mt * 40 + w * 5 + r) * (3 * D * 2) + n0 * 2 + lane * 4);
            TRC();
            const f32x2 st = row_stats(x, m0 + nt * 8 + w, lane);
            TRC();
            f32x2 c = {};
#pragma unroll
            for (int r = 0; r < 5; ++r) { c[0] += bflo(cv[r]); c[1] += bfhi(cv[r]); }
            *(LAS f32x2*)(lds + L_P1SCR + w * 512 + lane * 8) = c;
            if (lane < 2) *(LAS float*)(lds + L_P1SCR + 24576 + lane * 32 + w * 4) = lane ? st[1] : st[0];
        }
        if (tid == 0) *(LAS u64*)(lds + L_TAG) = nonce + 1;
        unsigned aoff[4];
#pragma unroll
        for (int ks = 0; ks < 4; ++ks) aoff[ks] = (wm * 64 + l31) * 128 + (((2 * ks + hh) ^ ((lane >> 1) & 7)) << 4);
        const unsigned boff = 16384 + 2 * hh * 1024 + wn * 256 + ((lane & 15) >> 2) * 64 + ((lane >> 4) & 1) * 32 + (lane & 3) * 8;
        const bool vtile = nt >= 10, isq = nt < 5;
        int dst[2], tstr[2], dv = 0;
        {
            const int kt = (m0 + wm * 64) >> 5;
            if (!vtile) {
#pragma unroll
                for (int g = 0; g < 2; ++g) {
                    const int G = (n0 - (isq ? 0 : D) + wn * 32) / 8 + 2 * g + hh, pr = (G * 205) >> 10, r5 = G - pr * 5;
                    tstr[g] = r5 == 2 ? 512 : 1024;
                    dst[g] = (r5 == 2 ? (isq ? W_QR : W_KR) + pr * 32768 : (isq ? W_QM : W_KM) + (2 * pr + (r5 > 2)) * 65536 + (r5 > 2 ? r5 - 3 : r5) * 512)
                             + kt * tstr[g] + l31 * 16;
                }
            } else {
                const int nn = n0 - 2 * D + wn * 32 + l31, hd = (nn * 205) >> 12;
                dv = nn - hd * 20;
                dst[0] = W_VT + hd * 90112 + kt * 1408 + (hh * 20 + dv) * 16; dst[1] = dst[0] + 704; tstr[0] = tstr[1] = 1408;
            }
            PIN(dst[0]); PIN(dst[1]); PIN(tstr[0]); PIN(tstr[1]); PIN(dv);
        }
        f32x16 acc[2] = {f32x16{}, f32x16{}};
        BAR();
        const u64 tag = uni64(*(LAS u64*)(lds + L_TAG));
        if (w == 7) {
            f32x2 c = {};
#pragma unroll
            for (int i = 0; i < 8; ++i) c += *(LAS f32x2*)(lds + L_P1SCR + i * 512 + lane * 8);
            st64(rws, W_CSP + (mt * 1920 + n0) * 4 + lane * 8, __builtin_bit_cast(u32x2, c));
            if (lane < 4) st128(rws, W_RS + (lane >> 1) * 8192 + (m0 + nt * 8) * 4 + (lane & 1) * 16, lds128(lds + L_P1SCR + 24576 + lane * 16));
        }
        const int tpub = w == 7 ? 1 : -1;
        auto hook = [&](int t) {
            if (t == tpub) {
                DRAIN();
                if (lane == 0) set_flag(ws, W_F0 + (mt * 16 + nt) * FL, tag);
            }
        };
        if (vtile) loop32<false>(lds, aoff, boff, acc, hook TR_PASS);
        else loop32<true>(lds, aoff, boff, acc, hook TR_PASS);
        STAMP(1);
        lds_ptr srs = lds + L_P1SCR + 12288 + wm * 256, scs = lds + L_P1SCR + 13312 + wn * 128;
        if (!vtile) {
            f32x4 c4[4]; float mean[2], r[2];
#pragma unroll
            for (int g = 0; g < 4; ++g) c4[g] = *(LAS const f32x4*)(scs + (g * 8 + hh * 4) * 4);
#pragma unroll
            for (int i = 0; i < 2; ++i) {
                mean[i] = *(LAS float*)(srs + (i * 32 + l31) * 4);
                r[i] = *(LAS float*)(srs + 512 + (i * 32 + l31) * 4) * (isq ? 0.22360679774997896f * 1.4426950408889634f : 1.0f);
            }
#pragma unroll
            for (int i = 0; i < 2; ++i)
#pragma unroll
                for (int e = 0; e < 16; ++e) acc[i][e] = r[i] * (acc[i][e] - mean[i] * c4[e >> 2][e & 3]);
        } else {
            const float cs = *(LAS float*)(scs + l31 * 4);
            f32x4 m4[8], r4[8];
#pragma unroll
            for (int g = 0; g < 8; ++g) { m4[g] = *(LAS const f32x4*)(srs + (g * 8 + hh * 4) * 4); r4[g] = *(LAS const f32x4*)(srs + 512 + (g * 8 + hh * 4) * 4); }
            SBAR();
#pragma unroll
            for (int i = 0; i < 2; ++i)
#pragma unroll
                for (int e = 0; e < 16; ++e) acc[i][e] = r4[i * 4 + (e >> 2)][e & 3] * (acc[i][e] - m4[i * 4 + (e >> 2)][e & 3] * cs);
            if (dv < 2) {
                const unsigned c = dv == 0 ? 0x3f803f80u : 0u;
#pragma unroll
                for (int i = 0; i < 2; ++i) st128(rws, dst[0] + i * 1408 + 640 + hh * 384, (u32x4){c, c, c, c});
            }
        }
#pragma unroll
        for (int i = 0; i < 2; ++i)
#pragma unroll
            for (int g = 0; g < 2; ++g)
                st128(rws, dst[g] + i * tstr[g], widen((u32x2){pk2(acc[i][8 * g], acc[i][8 * g + 1]), pk2(acc[i][8 * g + 2], acc[i][8 * g + 3])},
                                                       (u32x2){pk2(acc[i][8 * g + 4], acc[i][8 * g + 5]), pk2(acc[i][8 * g + 6], acc[i][8 * g + 7])}));
        DRAIN(); BAR();
        if (tid == 0) set_flag(ws, W_F1 + (nt * 16 + mt) * FL, tag);
    } else {
        const f32x2 st = row_stats(x, m0 + 120 + w, lane);
        if (lane < 2) st32(rws, W_RS + lane * 8192 + (m0 + 120 + w) * 4, lane ? st[1] : st[0]);
        float cs[8] = {};
        const int ch = tid % 216, rs = tid / 216;
        if (tid < 432) {
#pragma unroll 4
            for (int r = rs; r < 40; r += 2) {
                const u32x4 v = *(const u32x4*)(w1 + (mt * 40 + r) * (FF * 2) + ch * 16);
#pragma unroll
                for (int e = 0; e < 4; ++e) { cs[2 * e] += bflo(v[e]); cs[2 * e + 1] += bfhi(v[e]); }
            }
            *(LAS f32x4*)(lds + tid * 32) = (f32x4){cs[0], cs[1], cs[2], cs[3]};
            *(LAS f32x4*)(lds + tid * 32 + 16) = (f32x4){cs[4], cs[5], cs[6], cs[7]};
        }
        if (tid == 0) *(LAS u64*)(lds + L_TAG) = nonce + 1;
        DRAIN(); BAR();
        if (tid == 0) set_flag(ws, W_F0 + (mt * 16 + 15) * FL, nonce + 1);
        if (tid < 432) st128(rws, W_CS1 + mt * (FF * 4) + tid * 16, __builtin_bit_cast(u32x4, *(LAS f32x4*)(lds + tid * 16) + *(LAS f32x4*)(lds + 6912 + tid * 16)));
        DRAIN(); BAR();
        if (tid == 0) set_flag(ws, W_FW + mt * FL, nonce + 1);
    }
    STAMP(2); TR_SET(2);
    const u64 tag = uni64(*(LAS u64*)(lds + L_TAG));
    {
        const int wq = w & 3, kh = w >> 2;
        const int q0 = qb * 256 + wq * 64;
        BAR();
        u32x4 qm[2]; u32x2 qe[2], qo[2];
#pragma unroll
        for (int i = 0; i < 2; ++i) {
            qm[i] = ld128(rws, W_QM + head * 65536 + ((q0 >> 5) + i) * 1024 + lane * 16);
            const u32x2 qr = ld64(rws, W_QR + (head >> 1) * 32768 + (q0 + i * 32 + l31) * 16 + (head & 1) * 8);
            qe[i] = hh == 0 ? qr : (u32x2){0u, 0u}; qo[i] = hh == 1 ? qr : (u32x2){0u, 0u};
        }
        const unsigned vrow = l31 < 20 ? (hh * 20 + l31) * 16 : (l31 == 20 ? 640 : 656);
        const unsigned koff = lane * 16 + kh * 4096, roff = 8192 + lane * 16 + (head & 1) * 8 + kh * 2048, voff = 12288 + kh * 4 * 1408 + vrow;
        unsigned idx = 0xddddddddu; PIN(idx);
        f32x16 s0, s1, nm0, nm1, o0 = {}, o1 = {};
        u32x4 km, kn, vl, vh = {}; u32x2 kr;
        PIN(o0); PIN(o1); PIN(vh);
        BAR();
        STAMP(3);
        {
            km = lds128(lds + lane * 16); kr = lds64(lds + 8192 + lane * 16 + (head & 1) * 8);
            s0 = mfma8(kr, qe[0], mfma32(km, qm[0], f32x16{}));
            s1 = mfma8(kr, qe[1], mfma32(km, qm[1], f32x16{}));
            km = lds128(lds + koff); kr = lds64(lds + roff); vl = lds128(lds + voff);
            float a = s0[0], b = s1[0];
#pragma unroll
            for (int r = 1; r < 16; ++r) { a = fmaxf(a, s0[r]); b = fmaxf(b, s1[r]); }
            a = -half_max(a); b = -half_max(b);
#pragma unroll
            for (int r = 0; r < 16; ++r) { nm0[r] = a; nm1[r] = b; }
            PIN(nm0); PIN(nm1);
        }
        s0 = mfma8(kr, qe[0], mfma32(km, qm[0], nm0));
        s1 = f32x16{};
#define EXP4(X, i) do { X[i] = __builtin_amdgcn_exp2f(X[i]); X[i + 1] = __builtin_amdgcn_exp2f(X[i + 1]); \
                        X[i + 2] = __builtin_amdgcn_exp2f(X[i + 2]); X[i + 3] = __builtin_amdgcn_exp2f(X[i + 3]); PIN(X); } while (0)
#define TILE(T) do { \
        o1 = smfma(vh, hi8(s1), o1, idx); SBAR(); \
        vh = lds128(cur + voff + (T) * 1408 + 704); \
        kn = lds128(((T) < 3 ? cur : nxt) + koff + (((T) + 1) & 3) * 1024); \
        EXP4(s0, 0); SBAR(); \
        s1 = mfma32(km, qm[1], nm1); SBAR(); \
        EXP4(s0, 4); SBAR(); \
        s1 = mfma8(kr, ((T) & 1) ? qo[1] : qe[1], s1); SBAR(); \
        if ((T) & 1) kr = lds64(((T) < 3 ? cur : nxt) + roff + (((T) + 1) & 3) * 512); \
        EXP4(s0, 8); SBAR(); \
        o0 = smfma(vl, lo8(s0), o0, idx); SBAR(); \
        EXP4(s0, 12); SBAR(); \
        o0 = smfma(vh, hi8(s0), o0, idx); SBAR(); \
        EXP4(s1, 0); SBAR(); \
        s0 = mfma32(kn, qm[0], nm0); km = kn; SBAR(); \
        EXP4(s1, 4); SBAR(); \
        s0 = mfma8(kr, ((T) & 1) ? qe[0] : qo[0], s0); SBAR(); \
        EXP4(s1, 8); SBAR(); \
        o1 = smfma(vl, lo8(s1), o1, idx); SBAR(); \
        vl = lds128(((T) < 3 ? cur : nxt) + voff + (((T) + 1) & 3) * 1408); \
        EXP4(s1, 12); SBAR(); \
        } while (0)
        int sl = 0;
#pragma unroll 1
        for (int c = 0; c < 8; ++c) {
            const int sn = sl == 2 ? 0 : sl + 1;
            lds_ptr cur = lds + sl * AT_SLOT, nxt = lds + sn * AT_SLOT; sl = sn;
            TILE(0); TILE(1); TILE(2);
            BAR();
            TILE(3);
        }
        o1 = smfma(vh, hi8(s1), o1, idx);
        STAMP(4);
        {
            lds_ptr ex = lds + 2 * AT_SLOT + lane * 16;
#pragma unroll
            for (int g = 0; g < 3; ++g)
                *(LAS f32x4*)(ex + w * 3072 + g * 1024) = kh ? (f32x4){o0[4 * g], o0[4 * g + 1], o0[4 * g + 2], o0[4 * g + 3]}
                                                             : (f32x4){o1[4 * g], o1[4 * g + 1], o1[4 * g + 2], o1[4 * g + 3]};
            BAR();
            float o[12]; float chk = 0.f;
#pragma unroll
            for (int g = 0; g < 3; ++g) {
                const f32x4 p = *(LAS f32x4*)(ex + (w ^ 4) * 3072 + g * 1024);
#pragma unroll
                for (int e = 0; e < 4; ++e) { o[4 * g + e] = p[e] + (kh ? o1[4 * g + e] : o0[4 * g + e]); chk += fabsf(o[4 * g + e]); }
            }
            if (__any(!(chk < 1e30f))) {
                const u32x4 fq = kh ? qm[1] : qm[0]; const u32x2 fe = kh ? qe[1] : qe[0], fo = kh ? qo[1] : qo[0];
                f32x16 fn = {}, fa = {};
#pragma unroll 1
                for (int pass = 0; pass < 2; ++pass) {
                    float m = -INFINITY;
#pragma unroll 1
                    for (int t = 0; t < 64; ++t) {
                        const u32x4 a = ld128(rws, W_KM + head * 65536 + t * 1024 + lane * 16);
                        const u32x2 b = ld64(rws, W_KR + (head >> 1) * 32768 + (t >> 1) * 1024 + lane * 16 + (head & 1) * 8);
                        f32x16 s = mfma8(b, (t & 1) ? fo : fe, mfma32(a, fq, fn));
                        if (pass == 0) {
#pragma unroll
                            for (int r = 0; r < 16; ++r) m = fmaxf(m, s[r]);
                        } else {
#pragma unroll
                            for (int r = 0; r < 16; ++r) s[r] = __builtin_amdgcn_exp2f(s[r]);
                            fa = smfma(ld128(rws, W_VT + head * 90112 + t * 1408 + vrow), lo8(s), fa, idx);
                            fa = smfma(ld128(rws, W_VT + head * 90112 + t * 1408 + 704 + vrow), hi8(s), fa, idx);
                        }
                    }
                    m = -half_max(m);
#pragma unroll
                    for (int r = 0; r < 16; ++r) fn[r] = m;
                }
#pragma unroll
                for (int r = 0; r < 12; ++r) o[r] = fa[r];
            }
            auto lr = __builtin_amdgcn_permlane32_swap(__float_as_uint(o[8]), __float_as_uint(o[8]), false, false);
            const float inv = __builtin_amdgcn_rcpf(__uint_as_float(lr[1]));
            const int ob = W_O + (q0 >> 6) * 81920;
#pragma unroll
            for (int g = 0; g < 3; ++g) {
                const int k4 = head * 5 + 2 * g + hh;
                if (g < 2 || hh == 0)
                    st64(rws, ob + k4 * 512 + (((kh * 32 + l31) * 8) ^ ((k4 & 2) << 6)),
                         (u32x2){pk2(o[4 * g] * inv, o[4 * g + 1] * inv), pk2(o[4 * g + 2] * inv, o[4 * g + 3] * inv)});
            }
            DRAIN(); BAR();
            if (tid == 0) set_flag(ws, W_F2 + (qb * 32 + head) * FL, tag);
        }
    }
    STAMP(5); TR_SET(3);

    const int l15 = lane & 15, kg = lane >> 4;
    constexpr int RP = 336;
    const bool act = w < (cg < 6 ? 7 : 6);
    const int w1s = W_W1T + (t4 + (act ? w : 0)) * 40960;
    constexpr int WIN = 20;
    u32x4 wb[WIN];
    {
        const int mi = w & 3, kh = w >> 2;
        u32x4 xr[2];
#pragma unroll
        for (int r = 0; r < 2; ++r) xr[r] = *(const u32x4*)(x + (rb * 64 + lane) * (D * 2) + (cg * 10 + min(w + 8 * r, 9)) * 16);
        f32x4 acc[5] = {};
        const int k4 = kh * 8 + kg * 2;
        const unsigned aoff = k4 * 512 + (((mi * 16 + l15) * 8) ^ ((k4 & 2) << 6)), boff = L_B80 + kh * 5120 + vlin;
        u32x4 xa[2], wf[2][5];
        auto half = [&](int m, int r, int sl, bool domm) {
            lds_ptr ap = lds + sl * 16384 + r * 8192 + aoff, bp = lds + sl * 20480 + r * 10240 + boff;
#pragma unroll
            for (int j = 0; j < 5; ++j) {
                if (domm) { acc[j] = mfma16(wf[m][j], xa[m], acc[j]); SBAR(); }
                if (j == 0) xa[r] = cat(lds64(ap), lds64(ap + 512));
                if (j < 4) wf[r][j] = lds128(bp + j * 1024);
                if (j == 1) wf[r][4] = lds128(bp + 4096);
                SBAR();
            }
        };
        BAR();
        BAR();
        half(1, 0, 0, false);
#pragma unroll
        for (int p = 0; p < 5; ++p) {
#pragma unroll
            for (int k = 0; k < 2; ++k) wb[4 * p + k] = __builtin_amdgcn_raw_buffer_load_b128(rws, vlin + k * 1024, w1s + p * 4096, SC1);
            SBAR();
            half(0, 1, p & 3, true);
#pragma unroll
            for (int k = 2; k < 4; ++k) wb[4 * p + k] = __builtin_amdgcn_raw_buffer_load_b128(rws, vlin + k * 1024, w1s + p * 4096, SC1);
            BAR();
            if (p < 4) half(1, 0, (p + 1) & 3, true);
        }
#pragma unroll
        for (int j = 0; j < 5; ++j) acc[j] = mfma16(wf[1][j], xa[1], acc[j]);
        if (tid == 0) *(LAS unsigned*)(lds + L_CNT) = 0u;
        STAMP(6);
#pragma unroll
        for (int j = 0; j < 5; ++j) *(LAS f32x4*)(lds + (kh * 64 + mi * 16 + l15) * RP + (j * 16 + kg * 4) * 4) = acc[j];
        BAR();
#pragma unroll
        for (int r = 0; r < 2; ++r) {
            if (r == 0 || w < 2) {
                const int c8 = w + 8 * r;
                lds_ptr p = lds + lane * RP + c8 * 32;
                const f32x4 u0 = *(LAS f32x4*)p + *(LAS f32x4*)(p + 64 * RP), u1 = *(LAS f32x4*)(p + 16) + *(LAS f32x4*)(p + 64 * RP + 16);
                u32x4 hv;
                hv[0] = pk2(u0[0] + bflo(xr[r][0]), u0[1] + bfhi(xr[r][0])); hv[1] = pk2(u0[2] + bflo(xr[r][1]), u0[3] + bfhi(xr[r][1]));
                hv[2] = pk2(u1[0] + bflo(xr[r][2]), u1[1] + bfhi(xr[r][2])); hv[3] = pk2(u1[2] + bflo(xr[r][3]), u1[3] + bfhi(xr[r][3]));
                st128(rws, W_H + rb * 81920 + (cg * 10 + c8) * 1024 + lane * 16, hv);
                float s = 0.f, q = 0.f;
#pragma unroll
                for (int e = 0; e < 4; ++e) { const float a = bflo(hv[e]), b = bfhi(hv[e]); s += a + b; q += a * a + b * b; }
                *(LAS f32x2*)(lds + L_PERS + c8 * 512 + lane * 8) = (f32x2){s, q};
            }
        }
        __builtin_amdgcn_s_waitcnt(0x0F70);
        DRAIN(); BAR();
        if (tid == 0) set_flag(ws, W_F3 + (rb * 8 + cg) * FL, tag);
        if (w == 7) {
            f32x2 t = {};
#pragma unroll
            for (int c = 0; c < 10; ++c) t += *(LAS f32x2*)(lds + L_PERS + c * 512 + lane * 8);
            st64(rws, W_HST + (rb * 8 + cg) * 512 + lane * 8, __builtin_bit_cast(u32x2, t));
            DRAIN();
            if (lane == 0) set_flag(ws, W_F3S + (rb * 8 + cg) * FL, tag);
        }
    }
    STAMP(7); TR_SET(4);
    const int kp = kpos(cg, w);
    if (act) {
        const int nw = (t4 + w) * 32;
        lds_ptr scr = lds + L_CB + w * 256;
        wait_flags(ws, W_FW + l15 * FL, tag);
        float c = 0.f;
#pragma unroll
        for (int i = 0; i < 8; ++i) c += ld32f(rws, W_CS1 + ((hh * 8 + i) * FF + nw + l31) * 4);
        c = half_sum(c);
        const float bb = bflo(*(const uint16_t*)(b1 + (nw + l31) * 2));
        *(LAS float*)(scr + lane * 4) = hh ? bb : c;
        f32x16 acc[2] = {f32x16{}, f32x16{}};
        lds_ptr ap = lds + hh * 1024 + l31 * 16, ap2 = ap + 40960;
        u32x4 xa[4][2];
        BAR();
        BAR();
        if (w >= 4) { BARN(); BARN(); } else __builtin_amdgcn_s_setprio(1);
        u64 fs = 0; u32x2 hs[8];
#pragma unroll
        for (int k = 0; k < 2; ++k) { xa[k][0] = lds128(ap + k * 2048); xa[k][1] = lds128(ap + k * 2048 + 512); }
#pragma unroll
        for (int k = 0; k < 40; ++k) {
            if ((k == 6 || k == 14) && w < 4) BARN();
            if (k == 21) fs = ld_flag(ws, W_F3S + (rb * 8 + (lane & 7)) * FL);
            if (k == 30) {
                if (!__all(fs == tag)) wait_flags(ws, W_F3S + (rb * 8 + (lane & 7)) * FL, tag);
                __builtin_amdgcn_fence(__ATOMIC_ACQUIRE, "wavefront");
#pragma unroll
                for (int c = 0; c < 8; ++c) hs[c] = ld64(rws, W_HST + (rb * 8 + c) * 512 + lane * 8);
                SBAR();
            }
            lds_ptr np = k + 2 < 20 ? ap + (k + 2) * 2048 : ap2 + (k - 18) * 2048;
            acc[0] = mfma32(wb[k % WIN], xa[k & 3][0], acc[0]); SBAR();
            if (k + 2 < 40) { xa[(k + 2) & 3][0] = lds128(np); xa[(k + 2) & 3][1] = lds128(np + 512); SBAR(); }
            acc[1] = mfma32(wb[k % WIN], xa[k & 3][1], acc[1]); SBAR();
            if (k + WIN < 40) { wb[k % WIN] = __builtin_amdgcn_raw_buffer_load_b128(rws, vlin + (k & 3) * 1024, w1s + ((k + WIN) >> 2) * 4096, SC1); SBAR(); }
        }
        STAMP(8); TRC();
        if (lane == 0) __hip_atomic_fetch_add((LAS unsigned*)(lds + L_CNT), 1u, __ATOMIC_RELAXED, __HIP_MEMORY_SCOPE_WORKGROUP);
        __builtin_amdgcn_s_setprio(0);
        f32x4 c4[4], b4[4];
#pragma unroll
        for (int g = 0; g < 4; ++g) { c4[g] = *(LAS const f32x4*)(scr + (g * 8 + hh * 4) * 4); b4[g] = *(LAS const f32x4*)(scr + 128 + (g * 8 + hh * 4) * 4); }
        float st_r, st_m;
        {
            float su = 0.f, sq = 0.f;
#pragma unroll
            for (int c = 0; c < 8; ++c) { su = add1(su, __uint_as_float(hs[c][0])); sq = add1(sq, __uint_as_float(hs[c][1])); }
            st_m = mul1(su, 1.0f / D);
            const float var = fmaxf(fma1(sq, 1.0f / D, -st_m * st_m), 0.f);
            st_r = __builtin_amdgcn_rsqf(var + 1e-5f);
            st_r *= __builtin_amdgcn_rsqf(var * st_r * st_r + 1e-5f);
        }
        const auto rr = __builtin_amdgcn_permlane32_swap(__float_as_uint(st_r), __float_as_uint(st_r), false, false);
        const auto mm = __builtin_amdgcn_permlane32_swap(__float_as_uint(st_m), __float_as_uint(st_m), false, false);
#pragma unroll
        for (int i = 0; i < 2; ++i) {
            const f32x16 a = acc[i];
            const float r = __uint_as_float(rr[i]), mean = __uint_as_float(mm[i]);
            const float rm = -r * mean, k1 = -2.302208198f, k2 = -0.1029432397f;
            float z[16], y[16]; unsigned pk[8];
#pragma unroll
            for (int e = 0; e < 16; ++e) {
                z[e] = fma1(r, a[e], fma1(rm, c4[e >> 2][e & 3], b4[e >> 2][e & 3]));
                y[e] = mul1(z[e], fma1(mul1(z[e], z[e]), k2, k1));
            }
#pragma unroll
            for (int e = 0; e < 16; ++e) y[e] = __builtin_amdgcn_exp2f(y[e]);
            SBAR();
#pragma unroll
            for (int e = 0; e < 16; ++e) asm("v_add_f32 %0, 1.0, %0" : "+v"(y[e]));
#pragma unroll
            for (int e = 0; e < 16; ++e) y[e] = __builtin_amdgcn_rcpf(y[e]);
            SBAR();
#pragma unroll
            for (int e = 0; e < 8; ++e) pk[e] = pk2(mul1(z[2 * e], y[2 * e]), mul1(z[2 * e + 1], y[2 * e + 1]));
#pragma unroll
            for (int g = 0; g < 2; ++g)
                st128(rws, W_H1 + rb * 221184 + (kp * 4 + 2 * g + hh) * 1024 + (i * 32 + l31) * 16,
                      widen((u32x2){pk[4 * g], pk[4 * g + 1]}, (u32x2){pk[4 * g + 2], pk[4 * g + 3]}));
            SBAR();
        }
        TRC();
        if (w < 4) {
            DRAIN();
            if (lane == 0) set_flag(ws, W_F4 + (rb * 64 + kp) * FL, tag);
        }
    } else {
        BAR(); BAR(); BAR(); BAR();
    }
    if (blockIdx.x == 0 && tid == 0) set_flag(ws, W_NONCE, tag);
    STAMP(9); TR_SET(5);
    {
        const int mh = w & 1, kq = w >> 1;
        const unsigned aoff = (kq * 4 + kg) * 1024 + (mh * 32 + l15) * 16, boff = 16384 + kq * 5120 + vlin;
        f32x4 acc[2][5] = {};
        u32x4 xa[2][2], wf[2][5];
        auto tick = [&](int m, int r, int p, bool domm, bool dold) {
            lds_ptr ap = lds + ((p + 2) & 3) * P5_SLOT + aoff, bp = lds + ((p + 2) & 3) * P5_SLOT + boff;
#pragma unroll
            for (int j = 0; j < 10; ++j) {
                if (domm) { acc[j & 1][j >> 1] = mfma16(wf[m][j >> 1], xa[m][j & 1], acc[j & 1][j >> 1]); SBAR(); }
                if (dold) {
                    if (j == 0) { xa[r][0] = lds128(ap); xa[r][1] = lds128(ap + 256); }
                    if (j >= 1 && j <= 5) wf[r][j - 1] = lds128(bp + (j - 1) * 1024);
                    SBAR();
                }
            }
        };
        BAR();
        tick(1, 1, 0, false, true);
        BAR();
        tick(1, 0, 1, true, true);
        const int ppub = (act && w >= 4) ? 1 : -1;
#pragma unroll 1
        for (int p = 1; p < 13; p += 2) {
            BAR();
            tick(0, 1, p + 1, true, true);
            if (p == ppub) {
                DRAIN();
                if (lane == 0) set_flag(ws, W_F4 + (rb * 64 + kp) * FL, tag);
            }
            BAR();
            tick(1, 0, p + 2, true, true);
        }
        u32x4 hr[2], br[2]; int row[2], c8[2];
#pragma unroll
        for (int r = 0; r < 2; ++r) {
            const int u = min(tid + 512 * r, 639);
            row[r] = (u * 6554) >> 16; c8[r] = u - row[r] * 10;
            hr[r] = ld128(rws, W_H + rb * 81920 + (cg * 10 + c8[r]) * 1024 + row[r] * 16);
            br[r] = *(const u32x4*)(b2 + (cg * 10 + c8[r]) * 16);
        }
        if (kq < 2) tick(0, 0, 0, true, false);
        BAR();
        STAMP(10);
#pragma unroll
        for (int j = 0; j < 10; ++j) *(LAS f32x4*)(lds + (kq * 64 + mh * 32 + (j & 1) * 16 + l15) * RP + ((j >> 1) * 16 + kg * 4) * 4) = acc[j & 1][j >> 1];
        BAR();
#pragma unroll
        for (int r = 0; r < 2; ++r) {
            if (r == 0 || w < 2) {
                lds_ptr p = lds + row[r] * RP + c8[r] * 32;
                f32x4 u0 = *(LAS f32x4*)p, u1 = *(LAS f32x4*)(p + 16);
#pragma unroll
                for (int q = 1; q < 4; ++q) { u0 += *(LAS f32x4*)(p + q * 64 * RP); u1 += *(LAS f32x4*)(p + q * 64 * RP + 16); }
                u32x4 ov;
                ov[0] = pk2(u0[0] + bflo(hr[r][0]) + bflo(br[r][0]), u0[1] + bfhi(hr[r][0]) + bfhi(br[r][0]));
                ov[1] = pk2(u0[2] + bflo(hr[r][1]) + bflo(br[r][1]), u0[3] + bfhi(hr[r][1]) + bfhi(br[r][1]));
                ov[2] = pk2(u1[0] + bflo(hr[r][2]) + bflo(br[r][2]), u1[1] + bfhi(hr[r][2]) + bfhi(br[r][2]));
                ov[3] = pk2(u1[2] + bflo(hr[r][3]) + bflo(br[r][3]), u1[3] + bfhi(hr[r][3]) + bfhi(br[r][3]));
                *(u32x4*)(out + (rb * 64 + row[r]) * (D * 2) + (cg * 10 + c8[r]) * 16) = ov;
            }
        }
    }
    STAMP(11); TR_DUMP();
}
}

extern "C" void kernel_launch(void* const* d_in, const int* in_sizes, int n_in, void* d_out, int out_size, void* d_ws, size_t ws_size,
                              hipStream_t stream) {
    (void)hipFuncSetAttribute((const void*)k_block, hipFuncAttributeMaxDynamicSharedMemorySize, LDS_BYTES);
    k_block<<<256, 768, LDS_BYTES, stream>>>((const char*)d_in[0], (const char*)d_in[1], (char*)d_ws, (const char*)d_in[4], (const char*)d_in[2],
                                             (const char*)d_in[6], (const char*)d_in[5], (const char*)d_in[7], (char*)d_out);
}
```

```cpp
#include <hip/hip_runtime.h>
#include <stdint.h>
#include <type_traits>

#define WARM_W2 1
#define SLP 6
#define SLC 4
#define APRIO 0
#define WARM 0
#define TDR() DRAIN()
#define PRE1 3
#define OUTSC 0
#define FSL 1
#define GO1 1
#define FW 16
#define OT 0
#define LFIRST 0
#define XPRIO 1
#define EARLYA 0
#define NOCLOSE 0
#define HOIST 5
#define RB 0
#define KC 14
#define KP3 10
#define KP2 16
#define BD 7
#define YPUB 1
#define OWN 1
#define ROT1 0
namespace {
constexpr int S = 2048, D = 640, FF = 1728;
typedef __attribute__((ext_vector_type(16))) float f32x16;
typedef __attribute__((ext_vector_type(8))) float f32x8;
typedef __attribute__((ext_vector_type(4))) float f32x4;
typedef __attribute__((ext_vector_type(2))) float f32x2;
typedef __attribute__((ext_vector_type(8))) __bf16 bf16x8;
typedef __attribute__((ext_vector_type(16))) __bf16 bf16x16;
typedef __attribute__((ext_vector_type(2))) __bf16 bf16x2;
typedef __attribute__((ext_vector_type(4))) short s16x4;
typedef __attribute__((ext_vector_type(4))) unsigned u32x4;
typedef __attribute__((ext_vector_type(2))) unsigned u32x2;
typedef unsigned long long u64;
typedef __amdgpu_buffer_rsrc_t rsrc_t;
#define LAS __attribute__((address_space(3)))
#define GAS __attribute__((address_space(1)))
typedef LAS char* lds_ptr;

constexpr int W_NONCE = 0;
constexpr int FL = 128;
constexpr int W_F0 = 1024;
constexpr int W_F1 = W_F0 + 256 * FL;
constexpr int W_F2 = W_F1 + 256 * FL;
constexpr int W_F3 = W_F2 + 256 * FL;
constexpr int W_FR = W_F3 + 256 * FL;
constexpr int W_FW = W_FR + 256 * FL;
constexpr int W_F4 = W_FW + 16 * FL;
constexpr int W_RS = W_F4 + 2048 * FL;
constexpr int W_CSP = W_RS + 256 * 128;
constexpr int W_CS1 = W_CSP + 16 * 1920 * 4;
constexpr int W_HST = W_CS1 + 4 * FF * 4;
constexpr int W_KM = 1 << 20;
constexpr int W_QM = W_KM + 32 * 65536;
constexpr int W_KR = W_QM + 32 * 65536;
constexpr int W_QR = W_KR + 16 * 32768;
constexpr int VP = 10368;
constexpr int W_VT = W_QR + 16 * 32768;
constexpr int W_O = W_VT + 260 * VP;
constexpr int W_H = W_O + 32 * 81920;
constexpr int W_H1 = W_H + 32 * 81920;
constexpr int W_WOT = W_H1 + 32 * 221184;
constexpr int W_W2T = W_WOT + 8 * 102400;
constexpr int W_W1T = W_W2T + 8 * 276480;
constexpr int W_END = W_W1T + 54 * 40960;
static_assert(W_HST + 32 * 8 * 1024 <= W_KM, "control area");

constexpr int LDS_BYTES = 163840;
constexpr int L_P1SCR = 131072;
constexpr int L_TAG = L_P1SCR + 28672;
constexpr int AT_SLOT = 24576;
constexpr int L_STG = 147456;
constexpr int L_B80 = 73728;
constexpr int L_A9 = 155648;
constexpr int L_XW = 81920;
constexpr int P5_SLOT = 40960;
constexpr int L_CB = 2 * P5_SLOT + 36864;
constexpr int L_HS = 3 * P5_SLOT + 36864;
constexpr int L_CNT = L_HS + 1024;
constexpr int L_FULL = L_HS + 2048;
constexpr int CODE_BYTES = 32768;

#define STAMP(idx)

#define DMA_I(v, o, c) "buffer_load_dwordx4 " v ", %1, %2 offen offset:" #o c " lds\n\t"
#define DMA_HEAD "s_mov_b32 m0, %0\n\ts_nop 3\n\t"
#define DMA_OPS(l, r, s) "s"(__builtin_amdgcn_readfirstlane(l)), "s"(r), "s"(__builtin_amdgcn_readfirstlane(s))
__device__ __forceinline__ void dma1(rsrc_t r, unsigned so, unsigned l, unsigned a) { asm volatile(DMA_HEAD DMA_I("%3", 0, "") :: DMA_OPS(l, r, so), "v"(a) : "memory"); }
__device__ __forceinline__ void dma3s(rsrc_t r, unsigned so, unsigned l, unsigned a, unsigned b, unsigned c) {
    asm volatile(DMA_HEAD DMA_I("%3", 0, " sc1") DMA_I("%4", 1024, " sc1") DMA_I("%5", 2048, " sc1") :: DMA_OPS(l, r, so), "v"(a), "v"(b), "v"(c) : "memory"); }
__device__ __forceinline__ void dma4(rsrc_t r, unsigned so, unsigned l, unsigned a, unsigned b, unsigned c, unsigned d) {
    asm volatile(DMA_HEAD DMA_I("%3", 0, "") DMA_I("%4", 1024, "") DMA_I("%5", 2048, "") DMA_I("%6", 3072, "") :: DMA_OPS(l, r, so), "v"(a), "v"(b), "v"(c), "v"(d) : "memory"); }
template <int N> __device__ __forceinline__ void dmal(rsrc_t r, unsigned so, unsigned l, unsigned v) {
    if (N == 1) asm volatile(DMA_HEAD DMA_I("%3", 0, " sc1") :: DMA_OPS(l, r, so), "v"(v) : "memory");
    if (N == 2) asm volatile(DMA_HEAD DMA_I("%3", 0, " sc1") DMA_I("%3", 1024, " sc1") :: DMA_OPS(l, r, so), "v"(v) : "memory");
    if (N == 3) asm volatile(DMA_HEAD DMA_I("%3", 0, " sc1") DMA_I("%3", 1024, " sc1") DMA_I("%3", 2048, " sc1") :: DMA_OPS(l, r, so), "v"(v) : "memory");
    if (N == 4) asm volatile(DMA_HEAD DMA_I("%3", 0, " sc1") DMA_I("%3", 1024, " sc1") DMA_I("%3", 2048, " sc1") DMA_I("%3", 3072, " sc1") :: DMA_OPS(l, r, so), "v"(v) : "memory");
}
__device__ __forceinline__ void touch(rsrc_t r, unsigned so, unsigned l, unsigned v) {
    asm volatile(DMA_HEAD "buffer_load_dword %3, %1, %2 offen sc1 lds" :: DMA_OPS(l, r, so), "v"(v) : "memory"); }
#define BAR() asm volatile("s_waitcnt lgkmcnt(0)\n\ts_barrier" ::: "memory")
#define BARN() asm volatile("s_barrier" ::: "memory")
#define WB(n) asm volatile("s_waitcnt vmcnt(" #n ") lgkmcnt(0)\n\ts_barrier" ::: "memory")
#define TR_ARGS
#define TRC()
#define TR_PASS
#define TR_SET(ph)
#define DRAIN() asm volatile("s_waitcnt vmcnt(0)" ::: "memory")
__device__ __forceinline__ u32x4 lds128(lds_ptr p) { return *(LAS const u32x4*)p; }
__device__ __forceinline__ u32x2 lds64(lds_ptr p) { return *(LAS const u32x2*)p; }
__device__ __forceinline__ u32x2 ldstr(lds_ptr p) { return __builtin_bit_cast(u32x2, __builtin_amdgcn_ds_read_tr16_b64_v4i16((LAS s16x4*)p)); }
__device__ __forceinline__ u32x4 cat(u32x2 a, u32x2 b) { return (u32x4){a[0], a[1], b[0], b[1]}; }
__device__ __forceinline__ f32x16 mfma32(u32x4 a, u32x4 b, f32x16 c) {
    return __builtin_amdgcn_mfma_f32_32x32x16_bf16(__builtin_bit_cast(bf16x8, a), __builtin_bit_cast(bf16x8, b), c, 0, 0, 0);
}
__device__ __forceinline__ f32x4 mfma16(u32x4 a, u32x4 b, f32x4 c) {
    return __builtin_amdgcn_mfma_f32_16x16x32_bf16(__builtin_bit_cast(bf16x8, a), __builtin_bit_cast(bf16x8, b), c, 0, 0, 0);
}
__device__ __forceinline__ f32x16 mfma8(u32x2 a, u32x2 b, f32x16 c) {
    return __builtin_amdgcn_mfma_f32_32x32x8bf16_1k(__builtin_bit_cast(s16x4, a), __builtin_bit_cast(s16x4, b), c, 0, 0, 0);
}
__device__ __forceinline__ f32x16 smfma(u32x4 a, f32x8 b, f32x16 c, unsigned idx) {
    return __builtin_amdgcn_smfmac_f32_32x32x32_bf16(__builtin_bit_cast(bf16x8, a), __builtin_bit_cast(bf16x16, b), c, idx, 0, 0);
}
__device__ __forceinline__ f32x8 lo8(const f32x16& s) { return __builtin_shufflevector(s, s, 0, 1, 2, 3, 4, 5, 6, 7); }
__device__ __forceinline__ f32x8 hi8(const f32x16& s) { return __builtin_shufflevector(s, s, 8, 9, 10, 11, 12, 13, 14, 15); }
__device__ __forceinline__ unsigned pk2(float lo, float hi) { bf16x2 v = {(__bf16)lo, (__bf16)hi}; return __builtin_bit_cast(unsigned, v); }
__device__ __forceinline__ float bflo(unsigned u) { return __uint_as_float(u << 16); }
__device__ __forceinline__ float bfhi(unsigned u) { return __uint_as_float(u & 0xffff0000u); }
__device__ __forceinline__ float fma1(float a, float b, float c) { float d; asm("v_fma_f32 %0, %1, %2, %3" : "=v"(d) : "v"(a), "v"(b), "v"(c)); return d; }
__device__ __forceinline__ float add1(float a, float b) { float d; asm("v_add_f32 %0, %1, %2" : "=v"(d) : "v"(a), "v"(b)); return d; }
__device__ __forceinline__ float mul1(float a, float b) { float d; asm("v_mul_f32 %0, %1, %2" : "=v"(d) : "v"(a), "v"(b)); return d; }
__device__ __forceinline__ float half_sum(float v) {
    auto r = __builtin_amdgcn_permlane32_swap(__float_as_uint(v), __float_as_uint(v), false, false);
    return __uint_as_float(r[0]) + __uint_as_float(r[1]);
}
__device__ __forceinline__ float quad_sum(float v) {
    auto r = __builtin_amdgcn_permlane16_swap(__float_as_uint(v), __float_as_uint(v), false, false);
    v = add1(__uint_as_float(r[0]), __uint_as_float(r[1]));
    auto q = __builtin_amdgcn_permlane32_swap(__float_as_uint(v), __float_as_uint(v), false, false);
    return add1(__uint_as_float(q[0]), __uint_as_float(q[1]));
}
__device__ __forceinline__ float half_max(float v) {
    auto r = __builtin_amdgcn_permlane32_swap(__float_as_uint(v), __float_as_uint(v), false, false);
    return fmaxf(__uint_as_float(r[0]), __uint_as_float(r[1]));
}
template <int C> __device__ __forceinline__ float dpp(float v) { return __builtin_bit_cast(float, __builtin_amdgcn_mov_dpp(__builtin_bit_cast(int, v), C, 0xf, 0xf, true)); }
__device__ __forceinline__ float wave_sum(float v) {
    v += dpp<0xB1>(v); v += dpp<0x4E>(v); v += dpp<0x141>(v); v += dpp<0x128>(v);
    auto r = __builtin_amdgcn_permlane16_swap(__float_as_uint(v), __float_as_uint(v), false, false);
    return half_sum(__uint_as_float(r[0]) + __uint_as_float(r[1]));
}
__device__ __forceinline__ u32x4 widen(u32x2 a, u32x2 b) {
    auto r0 = __builtin_amdgcn_permlane32_swap(a[0], b[0], false, false);
    auto r1 = __builtin_amdgcn_permlane32_swap(a[1], b[1], false, false);
    return (u32x4){r0[0], r1[0], r0[1], r1[1]};
}
constexpr int SC1 = 16;
__device__ __forceinline__ void st128(rsrc_t r, int off, u32x4 v) { __builtin_amdgcn_raw_buffer_store_b128(v, r, off, 0, SC1); }
__device__ __forceinline__ void st32(rsrc_t r, int off, float v) { __builtin_amdgcn_raw_buffer_store_b32(__float_as_uint(v), r, off, 0, SC1); }
__device__ __forceinline__ void st64(rsrc_t r, int off, u32x2 v) { __builtin_amdgcn_raw_buffer_store_b64(v, r, off, 0, SC1); }
__device__ __forceinline__ u32x4 ld128(rsrc_t r, int off) { return __builtin_amdgcn_raw_buffer_load_b128(r, off, 0, SC1); }
__device__ __forceinline__ u32x2 ld64(rsrc_t r, int off) { return __builtin_amdgcn_raw_buffer_load_b64(r, off, 0, SC1); }
__device__ __forceinline__ float ld32f(rsrc_t r, int off) { return __uint_as_float(__builtin_amdgcn_raw_buffer_load_b32(r, off, 0, SC1)); }
__device__ __forceinline__ void set_flag(char* ws, int off, u64 tag) {
    __hip_atomic_store((GAS u64*)(ws + off), tag, __ATOMIC_RELAXED, __HIP_MEMORY_SCOPE_AGENT);
}
__device__ __forceinline__ void wait_flags(char* ws, int off, u64 tag) {
    u64 v; int n;
    asm volatile("s_mov_b32 %1, 40000\n"
                 "1:\n\tglobal_load_dwordx2 %0, %2, %3 sc1\n\ts_waitcnt vmcnt(0)\n\tv_cmp_ne_u64 vcc, %4, %0\n\ts_nop 4\n\ts_cbranch_vccz 2f\n\t"
                 "s_sleep 1\n\ts_sub_u32 %1, %1, 1\n\ts_cbranch_scc0 1b\n2:"
                 : "=&v"(v), "=&s"(n) : "v"(off), "s"(ws), "s"(tag) : "vcc", "scc", "memory");
}
__device__ __forceinline__ u64 ld_flag(char* ws, int off) { return __hip_atomic_load((GAS u64*)(ws + off), __ATOMIC_RELAXED, __HIP_MEMORY_SCOPE_AGENT); }
__device__ __forceinline__ u64 uni64(u64 v) {
    return ((u64)(unsigned)__builtin_amdgcn_readfirstlane((unsigned)(v >> 32)) << 32) | (unsigned)__builtin_amdgcn_readfirstlane((unsigned)v);
}
template <int SL> __device__ __forceinline__ void wait_lds(lds_ptr p, int want) {
    int v, n;
    asm volatile("s_mov_b32 %1, 100000\n"
                 "1:\n\tds_read_b32 %0, %2\n\ts_waitcnt lgkmcnt(0)\n\tv_cmp_gt_i32 vcc, %3, %0\n\ts_nop 4\n\ts_cbranch_vccz 2f\n\t"
                 "s_sleep %4\n\ts_sub_u32 %1, %1, 1\n\ts_cbranch_scc0 1b\n2:"
                 : "=&v"(v), "=&s"(n) : "v"((unsigned)(size_t)p), "s"(want), "i"(SL) : "vcc", "scc", "memory");
}
#define SBAR() __builtin_amdgcn_sched_barrier(0)
#define PIN(x) asm volatile("" : "+v"(x))

__device__ __forceinline__ float dot2(unsigned a, unsigned b, float c) {
    return __builtin_amdgcn_fdot2_f32_bf16(__builtin_bit_cast(bf16x2, a), __builtin_bit_cast(bf16x2, b), c, false);
}
__device__ __forceinline__ f32x2 row_stats(u32x4 a, u32x4 b) {
    float s0 = 0.f, s1 = 0.f, q0 = 0.f, q1 = 0.f;
#pragma unroll
    for (int e = 0; e < 4; ++e) { s0 = dot2(a[e], 0x3f803f80u, s0); q0 = dot2(a[e], a[e], q0); s1 = dot2(b[e], 0x3f803f80u, s1); q1 = dot2(b[e], b[e], q1); }
    const float mean = wave_sum(s0 + s1) * (1.0f / D);
    const float var = fmaxf(wave_sum(q0 + q1) * (1.0f / D) - mean * mean, 0.f);
    return (f32x2){mean, __builtin_amdgcn_rsqf(var + 1e-5f)};
}

template <bool CT, class Hook, class Pre>
__device__ __forceinline__ void loop32(lds_ptr lds, const unsigned (&aoff)[4], unsigned boff, f32x16 (&acc)[2], Hook hook, Pre pre TR_ARGS) {
    constexpr int ASTG = 32768, BSTG = 32768, astep = 4096;
    u32x4 xa[4][2], wf[4];
    auto mm = [&](int k, int i) { acc[i] = CT ? mfma32(wf[k], xa[k][i], acc[i]) : mfma32(xa[k][i], wf[k], acc[i]); SBAR(); };
    auto rdb = [&](lds_ptr bp, int k) { return cat(ldstr(bp + k * 4096), ldstr(bp + k * 4096 + 1024)); };
    auto half = [&](int m, int r, int sl) {
        lds_ptr a0 = lds + sl * ASTG + aoff[r], a1 = lds + sl * ASTG + aoff[r + 1], bp = lds + sl * BSTG + boff;
        mm(m, 0);
        wf[r] = rdb(bp, r); xa[r][0] = lds128(a0); SBAR();
        mm(m, 1);
        xa[r][1] = lds128(a0 + astep); wf[r + 1] = rdb(bp, r + 1); SBAR();
        mm(m + 1, 0);
        xa[r + 1][0] = lds128(a1); xa[r + 1][1] = lds128(a1 + astep); SBAR();
        mm(m + 1, 1);
    };
    if (CT) asm volatile("; c^t" ::: "memory"); else asm volatile("; c" ::: "memory");
#pragma unroll
    for (int k = 0; k < 2; ++k) { wf[k] = rdb(lds + boff, k); xa[k][0] = lds128(lds + aoff[k]); xa[k][1] = lds128(lds + aoff[k] + astep); }
    half(0, 2, 0);
    BAR();
#pragma unroll 1
    for (int t = 1; t < 10; ++t) {
        half(2, 0, t & 3);
        half(0, 2, t & 3);
        hook(t);
        BAR();
    }
    mm(2, 0);
    pre(); SBAR();
    mm(3, 0); mm(2, 1); mm(3, 1);
}

#define ATT_IO "+{v[24:39]}"(o0), "+{v[40:55]}"(o1), "+{v[56:71]}"(s0), "+{v[72:87]}"(s1), "+{v[136:139]}"(ka_), "+{v[140:143]}"(kb_), "+{v[144:145]}"(kra), "+{v[146:147]}"(krb), "+{v[148:151]}"(vla), "+{v[152:155]}"(vha), "+{v[156:159]}"(vlb), "+{v[160:163]}"(vhb)
#define ATT_IN "{v[88:103]}"(nm0), "{v[104:119]}"(nm1), "{v[120:123]}"(qm[0]), "{v[124:127]}"(qm[1]), "{v[128:129]}"(qe[0]), "{v[130:131]}"(qo[0]), "{v[132:133]}"(qe[1]), "{v[134:135]}"(qo[1]), "{v164}"(idx)
#define ATT_LOOP \
    ".Latt_loop:\n\tv_exp_f32 v56, v56\n\tv_exp_f32 v57, v57\n\tv_smfmac_f32_32x32x32_bf16 v[40:55], v[160:163], v[80:87], v164\n\t" \
    "ds_read_b128 v[140:143], %[ka] offset:1024\n\tv_exp_f32 v58, v58\n\tv_exp_f32 v59, v59\n\tv_exp_f32 v60, v60\n\t" \
    "v_mfma_f32_32x32x16_bf16 v[72:87], v[136:139], v[124:127], v[104:119]\n\tds_read_b128 v[156:159], %[va] offset:1408\n\tv_exp_f32 v61, v61\n\t" \
    "v_exp_f32 v62, v62\n\tv_exp_f32 v63, v63\n\tv_mfma_f32_32x32x8_bf16 v[72:87], v[144:145], v[132:133], v[72:87]\n\t" \
    "ds_read_b128 v[160:163], %[va] offset:2112\n\tv_exp_f32 v64, v64\n\tv_exp_f32 v65, v65\n\tv_exp_f32 v66, v66\n\tv_exp_f32 v67, v67\n\t" \
    "v_smfmac_f32_32x32x32_bf16 v[24:39], v[148:151], v[56:63], v164\n\tv_exp_f32 v68, v68\n\tv_exp_f32 v69, v69\n\tv_exp_f32 v70, v70\n\t" \
    "v_exp_f32 v71, v71\n\tv_exp_f32 v72, v72\n\tv_exp_f32 v73, v73\n\tv_smfmac_f32_32x32x32_bf16 v[24:39], v[152:155], v[64:71], v164\n\t" \
    "s_waitcnt lgkmcnt(0)\n\tv_exp_f32 v74, v74\n\tv_exp_f32 v75, v75\n\tv_exp_f32 v76, v76\n\t" \
    "v_mfma_f32_32x32x16_bf16 v[56:71], v[140:143], v[120:123], v[88:103]\n\tv_exp_f32 v77, v77\n\tv_exp_f32 v78, v78\n\tv_exp_f32 v79, v79\n\t" \
    "v_mfma_f32_32x32x8_bf16 v[56:71], v[144:145], v[130:131], v[56:71]\n\tv_exp_f32 v80, v80\n\tv_exp_f32 v81, v81\n\tv_exp_f32 v82, v82\n\t" \
    "v_exp_f32 v83, v83\n\tv_smfmac_f32_32x32x32_bf16 v[40:55], v[148:151], v[72:79], v164\n\tv_exp_f32 v84, v84\n\tv_exp_f32 v85, v85\n\t" \
    "v_exp_f32 v86, v86\n\tv_exp_f32 v87, v87\n\tv_exp_f32 v56, v56\n\tv_exp_f32 v57, v57\n\t" \
    "v_smfmac_f32_32x32x32_bf16 v[40:55], v[152:155], v[80:87], v164\n\tds_read_b128 v[136:139], %[ka] offset:2048\n\t" \
    "ds_read_b64 v[146:147], %[ra] offset:1024\n\tv_exp_f32 v58, v58\n\tv_exp_f32 v59, v59\n\tv_exp_f32 v60, v60\n\t" \
    "v_mfma_f32_32x32x16_bf16 v[72:87], v[140:143], v[124:127], v[104:119]\n\tds_read_b128 v[148:151], %[va] offset:2816\n\tv_exp_f32 v61, v61\n\t" \
    "v_exp_f32 v62, v62\n\tv_exp_f32 v63, v63\n\tv_mfma_f32_32x32x8_bf16 v[72:87], v[144:145], v[134:135], v[72:87]\n\t" \
    "ds_read_b128 v[152:155], %[va] offset:3520\n\tv_exp_f32 v64, v64\n\tv_exp_f32 v65, v65\n\tv_exp_f32 v66, v66\n\tv_exp_f32 v67, v67\n\t" \
    "v_smfmac_f32_32x32x32_bf16 v[24:39], v[156:159], v[56:63], v164\n\tv_exp_f32 v68, v68\n\tv_exp_f32 v69, v69\n\tv_exp_f32 v70, v70\n\t" \
    "v_exp_f32 v71, v71\n\tv_exp_f32 v72, v72\n\tv_exp_f32 v73, v73\n\tv_smfmac_f32_32x32x32_bf16 v[24:39], v[160:163], v[64:71], v164\n\t" \
    "s_waitcnt lgkmcnt(0)\n\tv_exp_f32 v74, v74\n\tv_exp_f32 v75, v75\n\tv_exp_f32 v76, v76\n\t" \
    "v_mfma_f32_32x32x16_bf16 v[56:71], v[136:139], v[120:123], v[88:103]\n\tv_exp_f32 v77, v77\n\tv_exp_f32 v78, v78\n\tv_exp_f32 v79, v79\n\t" \
    "v_mfma_f32_32x32x8_bf16 v[56:71], v[146:147], v[128:129], v[56:71]\n\tv_exp_f32 v80, v80\n\tv_exp_f32 v81, v81\n\tv_exp_f32 v82, v82\n\t" \
    "v_exp_f32 v83, v83\n\tv_smfmac_f32_32x32x32_bf16 v[40:55], v[156:159], v[72:79], v164\n\tv_exp_f32 v84, v84\n\tv_exp_f32 v85, v85\n\t" \
    "v_exp_f32 v86, v86\n\tv_exp_f32 v87, v87\n\tv_exp_f32 v56, v56\n\tv_exp_f32 v57, v57\n\t" \
    "v_smfmac_f32_32x32x32_bf16 v[40:55], v[160:163], v[80:87], v164\n\tds_read_b128 v[140:143], %[ka] offset:3072\n\tds_read_b64 v[22:23], %[wa]\n\t" \
    "ds_read_b32 %[pv], %[pa]\n\tv_exp_f32 v58, v58\n\tv_exp_f32 v59, v59\n\tv_exp_f32 v60, v60\n\t" \
    "v_mfma_f32_32x32x16_bf16 v[72:87], v[136:139], v[124:127], v[104:119]\n\tds_read_b128 v[156:159], %[va] offset:4224\n\tv_exp_f32 v61, v61\n\t" \
    "v_exp_f32 v62, v62\n\tv_exp_f32 v63, v63\n\tv_mfma_f32_32x32x8_bf16 v[72:87], v[146:147], v[132:133], v[72:87]\n\t" \
    "ds_read_b128 v[160:163], %[va] offset:4928\n\tv_exp_f32 v64, v64\n\tv_exp_f32 v65, v65\n\tv_exp_f32 v66, v66\n\tv_exp_f32 v67, v67\n\t" \
    "v_smfmac_f32_32x32x32_bf16 v[24:39], v[148:151], v[56:63], v164\n\tv_exp_f32 v68, v68\n\tv_exp_f32 v69, v69\n\tv_exp_f32 v70, v70\n\t" \
    "v_exp_f32 v71, v71\n\tv_exp_f32 v72, v72\n\tv_exp_f32 v73, v73\n\tv_smfmac_f32_32x32x32_bf16 v[24:39], v[152:155], v[64:71], v164\n\t" \
    "s_waitcnt lgkmcnt(0)\n\tv_exp_f32 v74, v74\n\tv_exp_f32 v75, v75\n\tv_exp_f32 v76, v76\n\t" \
    "v_mfma_f32_32x32x16_bf16 v[56:71], v[140:143], v[120:123], v[88:103]\n\ts_add_u32 %[c], %[c], 1\n\tv_mov_b32 %[t], %[c]\n\t" \
    "ds_write_b32 %[ma], %[t]\n\tv_min_i32 v22, v22, v23\n\tv_exp_f32 v77, v77\n\tv_exp_f32 v78, v78\n\tv_exp_f32 v79, v79\n\t" \
    "v_mfma_f32_32x32x8_bf16 v[56:71], v[146:147], v[130:131], v[56:71]\n\tv_readfirstlane_b32 %[s0], %[pv]\n\tv_readfirstlane_b32 %[s1], v22\n\t" \
    "s_cmp_eq_u32 %[c], 3\n\ts_cselect_b32 %[d], %[w5], %[st]\n\ts_cmp_eq_u32 %[c], 6\n\ts_cselect_b32 %[d], %[w2], %[d]\n\tv_exp_f32 v80, v80\n\t" \
    "v_exp_f32 v81, v81\n\tv_exp_f32 v82, v82\n\tv_exp_f32 v83, v83\n\tv_smfmac_f32_32x32x32_bf16 v[40:55], v[148:151], v[72:79], v164\n\t" \
    "v_add_u32 %[ka], %[d], %[ka]\n\tv_add_u32 %[ra], %[d], %[ra]\n\tv_add_u32 %[va], %[d], %[va]\n\ts_add_u32 %[s0], %[s0], %[kh]\n\ts_setprio 0\n\t" \
    "s_cmp_ge_i32 %[s0], %[c]\n\ts_cbranch_scc0 .Latt_np\n\ts_setprio 1\n\t.Latt_np:\n\tv_exp_f32 v84, v84\n\tv_exp_f32 v85, v85\n\tv_exp_f32 v86, v86\n\t" \
    "v_exp_f32 v87, v87\n\ts_min_u32 %[s0], %[c], 7\n\ts_cmp_gt_i32 %[s1], %[s0]\n\ts_cbranch_scc1 .Latt_go\n\ts_mov_b32 %[d], 100000\n\t.Latt_spin:\n\t" \
    "s_sleep 4\n\tds_read_b64 v[22:23], %[wa]\n\ts_waitcnt lgkmcnt(0)\n\tv_min_i32 v22, v22, v23\n\ts_sub_u32 %[d], %[d], 1\n\t" \
    "s_cbranch_scc1 .Latt_go\n\tv_readfirstlane_b32 %[s1], v22\n\ts_nop 3\n\ts_cmp_gt_i32 %[s1], %[s0]\n\ts_cbranch_scc0 .Latt_spin\n\t.Latt_go:\n\t" \
    "v_exp_f32 v56, v56\n\tv_exp_f32 v57, v57\n\tv_smfmac_f32_32x32x32_bf16 v[40:55], v[152:155], v[80:87], v164\n\t" \
    "ds_read_b128 v[136:139], %[ka] offset:0\n\tds_read_b64 v[144:145], %[ra] offset:0\n\tv_exp_f32 v58, v58\n\tv_exp_f32 v59, v59\n\t" \
    "v_exp_f32 v60, v60\n\tv_mfma_f32_32x32x16_bf16 v[72:87], v[140:143], v[124:127], v[104:119]\n\tds_read_b128 v[148:151], %[va] offset:0\n\t" \
    "v_exp_f32 v61, v61\n\tv_exp_f32 v62, v62\n\tv_exp_f32 v63, v63\n\tv_mfma_f32_32x32x8_bf16 v[72:87], v[146:147], v[134:135], v[72:87]\n\t" \
    "ds_read_b128 v[152:155], %[va] offset:704\n\tv_exp_f32 v64, v64\n\tv_exp_f32 v65, v65\n\tv_exp_f32 v66, v66\n\tv_exp_f32 v67, v67\n\t" \
    "v_smfmac_f32_32x32x32_bf16 v[24:39], v[156:159], v[56:63], v164\n\tv_exp_f32 v68, v68\n\tv_exp_f32 v69, v69\n\tv_exp_f32 v70, v70\n\t" \
    "v_exp_f32 v71, v71\n\tv_exp_f32 v72, v72\n\tv_exp_f32 v73, v73\n\tv_smfmac_f32_32x32x32_bf16 v[24:39], v[160:163], v[64:71], v164\n\t" \
    "s_waitcnt lgkmcnt(0)\n\tv_exp_f32 v74, v74\n\tv_exp_f32 v75, v75\n\tv_exp_f32 v76, v76\n\t" \
    "v_mfma_f32_32x32x16_bf16 v[56:71], v[136:139], v[120:123], v[88:103]\n\tv_exp_f32 v77, v77\n\tv_exp_f32 v78, v78\n\tv_exp_f32 v79, v79\n\t" \
    "v_mfma_f32_32x32x8_bf16 v[56:71], v[144:145], v[128:129], v[56:71]\n\tv_exp_f32 v80, v80\n\tv_exp_f32 v81, v81\n\tv_exp_f32 v82, v82\n\t" \
    "v_exp_f32 v83, v83\n\tv_smfmac_f32_32x32x32_bf16 v[40:55], v[156:159], v[72:79], v164\n\tv_exp_f32 v84, v84\n\tv_exp_f32 v85, v85\n\t" \
    "v_exp_f32 v86, v86\n\tv_exp_f32 v87, v87\n\ts_cmp_lt_u32 %[c], 8\n\ts_cbranch_scc1 .Latt_loop\n\ts_setprio 0\n\t"

__device__ __forceinline__ int kpos(int g, int j) { return j < 4 ? g * 4 + j : 28 + (g < 6 ? 3 * g : 6 + 2 * g) + j; }

__global__ __launch_bounds__(768, 3) void k_block(const char* __restrict__ x, const char* __restrict__ wqkv, char* __restrict__ ws,
                                                  const char* __restrict__ w1, const char* __restrict__ wo, const char* __restrict__ w2,
                                                  const char* __restrict__ b1, const char* __restrict__ b2, char* __restrict__ out) {
    extern __shared__ __attribute__((aligned(16))) char smem[];
    lds_ptr lds = (lds_ptr)smem;
    const unsigned lds0 = (unsigned)(uintptr_t)smem;
    const char* pc;
    asm volatile("s_getpc_b64 %0" : "=s"(pc));
    const int tid = threadIdx.x, lane = tid & 63, w = __builtin_amdgcn_readfirstlane(tid >> 6);
    const int hh = lane >> 5, l31 = lane & 31;
    const int xcd = blockIdx.x & 7, jx = blockIdx.x >> 3;
    const bool gemm = jx < 30;
    const int rem = (xcd & 1) * 30 + jx;
    const int mt = gemm ? (xcd >> 1) * 4 + (rem & 3) : xcd * 2 + jx - 30, nt = gemm ? rem >> 2 : 15;
    const int m0 = mt * 128, n0 = nt * 128;
    const rsrc_t rws = __builtin_amdgcn_make_buffer_rsrc(ws, 0, W_END, 0x00020000);
    const int head = xcd * 4 + (jx >> 3), qb = jx & 7;
    const int rb = head, cg = qb;
    const int t4 = cg < 6 ? cg * 7 : 6 + cg * 6;
    const unsigned vlin = lane * 16;
    STAMP(0);
#define TR_DUMP()

    if (__builtin_expect(w >= 8, LFIRST)) {
        const int lw = w - 8;
        if (gemm) {
            const rsrc_t rx = __builtin_amdgcn_make_buffer_rsrc((void*)x, 0, S * D * 2, 0x00020000);
            const rsrc_t rw = __builtin_amdgcn_make_buffer_rsrc((void*)wqkv, 0, D * 3 * D * 2, 0x00020000);
            unsigned va[4], vb[4];
#pragma unroll
            for (int p = 0; p < 4; ++p) {
                const int row = p * 8 + (lane >> 3);
                va[p] = row * (D * 2) + (((lane & 7) ^ ((row >> 1) & 7)) << 4) - p * 1024;
                vb[p] = (p * 4 + ((lane >> 2) & 3)) * (3 * D * 2) + (lane >> 4) * 64 + (lane & 3) * 16 - p * 1024;
            }
            const int sa = (m0 + lw * 32) * (D * 2), sb = lw * 16 * (3 * D * 2) + n0 * 2;
            int kt = ROT1;
            auto issue = [&](int t) {
                const unsigned slot = lds0 + (t & 3) * 32768 + lw * 4096;
                dma4(rx, sa + kt * 128, slot, va[0], va[1], va[2], va[3]);
                dma4(rw, sb + kt * 64 * (3 * D * 2), slot + 16384, vb[0], vb[1], vb[2], vb[3]);
                kt = kt == 9 ? 0 : kt + 1;
            };
#pragma unroll 1
            for (int t = 0; t < 4; ++t) { TRC(); issue(t); if (t == PRE1 - 1) { STAMP(10); if (PRE1 == 2) WB(8); else WB(16); STAMP(11); } }
#pragma unroll 1
            for (int t = 1; t <= 4; ++t) { WB(16); issue(t + 3); }
            WB(16);
            const unsigned fl = L_P1SCR + 16384;
            const int f0 = W_F0 + ((lane & 16) ? (lane & 15) * 16 + nt : mt * 16 + (lane & 15)) * FL;
            if (lw == 0) dmal<1>(rws, 0, lds0 + fl, hh ? W_END : f0);
            issue(8);
            WB(16);
            issue(9);
            WB(16);
            const u64 tag = uni64(*(LAS u64*)(lds + L_TAG));
            if (!__all(*(LAS u64*)(lds + fl + l31 * 16) == tag)) wait_flags(ws, f0, tag);
            if (lw == 1) dmal<1>(rws, W_RS + mt * 2048, lds0 + L_P1SCR + 12288, (l31 >> 1) * 128 + hh * 32 + (l31 & 1) * 16);
            dmal<1>(rws, W_CSP + (lw * 4 * 1920 + n0) * 4, lds0 + L_P1SCR + 4096 + lw * 2048, hh * 7680 + l31 * 16);
            dmal<1>(rws, W_CSP + ((lw * 4 + 2) * 1920 + n0) * 4, lds0 + L_P1SCR + 4096 + lw * 2048 + 1024, hh * 7680 + l31 * 16);
            WB(10);
            WB(0);
            float c = 0.f;
#pragma unroll
            for (int i = 0; i < 8; ++i) c += *(LAS float*)(lds + L_P1SCR + 4096 + (hh * 8 + i) * 512 + (lw * 32 + l31) * 4);
            *(LAS float*)(lds + L_P1SCR + 13312 + (lw * 32 + l31) * 4) = half_sum(c);
            BAR();
        }
        if (tid < 528) *(LAS int*)(lds + L_FULL + (tid - 512) * 4) = 0;
        unsigned vv[2][3];
#pragma unroll
        for (int p = 0; p < 6; ++p) {
            const int u = (((lw + (p / 3) * 2) & 3) * 3 + p % 3) * 64 + lane, sb = (u * 1490) >> 16, r = u - sb * 44;
            vv[p / 3][p % 3] = (2 * sb + (r >= 20 && r < 40)) * VP + (r < 20 ? head * 20 + r : r < 40 ? head * 20 + r - 20 : 600 + r) * 16 + 2048 - (p % 3) * 1024;
            PIN(vv[p / 3][p % 3]);
        }
        const unsigned stg = L_STG + (lw & 1) * 5120;
        const int un = blockIdx.x * 4 + (lw & 1) * 2;
        int rt_dst[2]; unsigned rt_rd, rt_step, rt_hi, v[5];
        const char* src; int sstep;
        if (un < 592) {
            const bool two = un >= 160;
            const int u2 = two ? un - 160 : un, ks = u2 >> 3, g = u2 & 7;
            src = (two ? w2 + ks * 32 * (D * 2) : wo + (ks < 16 ? ks * 40 : ks * 160 - 2544) * (D * 2)) + g * 160; sstep = 160;
            const int sh = two ? 5 : ks < 16 ? 4 : 2;
#pragma unroll
            for (int p = 0; p < 5; ++p) {
                const int u = p * 64 + lane, pos = (u * 6554) >> 16, c = u - pos * 10;
                const int k = (pos & 0x13) | ((pos & 4) << 1) | ((pos & 8) >> 1);
                v[p] = ((k >> sh) * 20 + (k & ((1 << sh) - 1))) * (D * 2) + c * 16 - (p & 3) * 1024;
            }
            const int kg = ks < 42 ? ks / 7 : (ks - 6) / 6;
            rt_dst[0] = two ? W_W2T + g * 276480 + kpos(kg, ks - (kg < 6 ? kg * 7 : 6 + kg * 6)) * 5120 : W_WOT + g * 102400 + ks * 5120;
            rt_dst[1] = rt_dst[0] + (two ? 276480 : 102400);
            rt_rd = stg + ((lane >> 5) * 16 + ((lane >> 4) & 1) * 4 + ((lane & 15) >> 2)) * 160 + (lane & 3) * 8; rt_step = 32; rt_hi = 1280;
        } else {
            const int u2 = un - 592;
            src = w1 + (u2 & 7) * 80 * (FF * 2) + (u2 >> 3) * 64; sstep = 80 * (FF * 2);
#pragma unroll
            for (int p = 0; p < 5; ++p) v[p] = (p * 16 + (lane >> 2)) * (FF * 2) + (lane & 3) * 16 - (p & 3) * 1024;
            rt_dst[0] = W_W1T + u2 * 5120; rt_dst[1] = rt_dst[0] + 5120;
            rt_rd = stg + hh * 512 + ((lane & 15) >> 2) * 64 + ((lane >> 4) & 1) * 32 + (lane & 3) * 8; rt_step = 1024; rt_hi = 256;
        }
#pragma unroll
        for (int p = 0; p < 5; ++p) PIN(v[p]);
        PIN(rt_rd);
        __builtin_amdgcn_s_setprio(2);
        if (!gemm) { BAR(); BAR(); }
        const u64 tag = uni64(*(LAS u64*)(lds + L_TAG));
        const u32x2 tagv = __builtin_bit_cast(u32x2, tag);
        auto bar_signal = [&](int line) {
            const int off = lw == 1 && lane < 16 ? line + lane * 8 : W_END;
            asm volatile("s_waitcnt lgkmcnt(0)\n\ts_barrier\n\tbuffer_store_dwordx2 %0, %1, %2, 0 offen sc1" :: "v"(tagv), "v"(off), "s"(rws) : "memory");
        };
        if (gemm) {
            bar_signal(W_F1 + (nt * 16 + mt) * FL);
        }
        STAMP(1); TR_SET(2);
        const int f1 = W_F1 + (5 + (lane >> 5) * 5 + ((head * 20 + ((lane >> 4) & 1) * 19) >> 7)) * 16 * FL;
        if (lw == 0) {
            if (!gemm) __builtin_amdgcn_s_sleep(60);
            const int l = min(lane, 11), ty = l >> 2;
            wait_flags(ws, W_F1 + (((ty == 2 ? 0 : 5 + ty * 5) + ((head * 20 + (l & 1) * 19) >> 7)) * 16 + (ty == 2 ? qb * 2 : 0) + ((l >> 1) & 1)) * FL, tag);
        }
        BAR();
        STAMP(2);
        auto copy5 = [&](int src5, unsigned dst) {
            dmal<4>(rws, src5 + lw * 5120, dst + lw * 5120, vlin);
            dmal<1>(rws, src5 + lw * 5120 + 4096, dst + lw * 5120 + 4096, vlin);
        };
        const int w2s = W_W2T + cg * 276480;
        u32x4 xreg[16], wreg[20];
        {
            auto issue = [&](int c, int slot, int h2) {
                const int q = lw + 2 * h2;
                const unsigned dst = lds0 + slot * AT_SLOT;
                dmal<2>(rws, W_KM + head * 65536 + c * 8192 + q * 2048, dst + q * 2048, vlin);
                dmal<1>(rws, W_KR + (head >> 1) * 32768 + c * 4096 + q * 1024, dst + 8192 + q * 1024, vlin);
                dma3s(rws, W_VT - 2048 + c * 32 * VP, dst + 12288 + q * 3072, vv[h2][0], vv[h2][1], vv[h2][2]);
            };
            issue(0, 3, 0);
            WB(0);
            if (lw < 2) {
                wait_flags(ws, f1 + (lane & 15) * FL, tag);
#pragma unroll 1
                for (int c = 1; c < 8; ++c) {
                    if (c >= 6) wait_lds<16>(lds + L_FULL + 32 + (lane & 7) * 4, c - 2);
#pragma unroll
                    for (int q = 0; q < 2; ++q) issue(c, c < 3 ? c + 3 : c < 6 ? c - 3 : c - 6, q);
                    DRAIN();
                    *(volatile LAS int*)(lds + L_FULL + lw * 4) = c + 1;
                    TRC();
                }
                wait_lds<16>(lds + L_FULL + 8 + (lane & 1) * 4, 1);
            } else {
                const rsrc_t rs = __builtin_amdgcn_make_buffer_rsrc((void*)src, 0, 160 * FF * 2, 0x00020000);
                wait_lds<4>(lds + L_FULL + (lane & 1) * 4, 2);
#pragma unroll 1
                for (int u = 0; u < 2; ++u) {
                    dma4(rs, u * sstep, lds0 + stg, v[0], v[1], v[2], v[3]);
                    dma1(rs, u * sstep, lds0 + stg + 4096, v[4]);
                    DRAIN();
#pragma unroll
                    for (int p = 0; p < 5; ++p) {
                        const u32x4 f = cat(ldstr(lds + rt_rd + p * rt_step), ldstr(lds + rt_rd + p * rt_step + rt_hi));
                        st128(rws, (u ? rt_dst[1] : rt_dst[0]) + p * 1024 + lane * 16, f);
                    }
                }
                DRAIN();
                TRC();
                if (lane < 8) set_flag(ws, W_FR + (blockIdx.x * 2 + lw - 2) * 64 + lane * 8, tag);
                __builtin_amdgcn_s_sleep(24);
#pragma unroll 1
                for (int q = 0; q < 4; ++q) wait_flags(ws, W_FR + ((lw - 2) * 256 + q * 64 + lane) * 64, tag);
                wait_flags(ws, W_FW + min(lane, 13) * FL, tag);
                TRC();
                *(volatile LAS int*)(lds + L_FULL + lw * 4) = 1;
                wait_lds<4>(lds + L_FULL + 8 + (lane & 1) * 4, 1);
                const int q8 = (rb & 3) * 2 + lw - 2;
                const rsrc_t rc = __builtin_amdgcn_make_buffer_rsrc((void*)pc, 0, CODE_BYTES, 0x00020000);
                touch(rc, q8 * 8192, lds0 + L_HS, lane * 128);
                touch(__builtin_amdgcn_make_buffer_rsrc((void*)(lw == 2 ? b1 : b2), 0, lw == 2 ? FF * 2 : D * 2, 0x00020000), 0, lds0 + L_HS, lane * 128);
                TRC();
                wait_lds<16>(lds + L_FULL + 32 + (lane & 7) * 4, 3);
                TRC();
#pragma unroll 1
                for (int p = 0; p < 10; ++p) dmal<4>(rws, W_WOT + cg * 102400 + (lw - 2) * 40960 + p * 4096, lds0 + L_B80 + (lw - 2) * 40960 + p * 4096, vlin);
            }
#pragma unroll
            for (int p = 0; p < 20; ++p) wreg[p] = __builtin_amdgcn_raw_buffer_load_b128(rws, vlin, w2s + (((RB ? (p < 15 ? 2 * (p / 5) + 1 : 6) : p / 5) + cg) & 7) * 20480 + lw * 5120 + (p % 5) * 1024, SC1);
#pragma unroll
            for (int p = 0; p < 16; ++p) xreg[p] = __builtin_amdgcn_raw_buffer_load_b128(rws, vlin, W_W1T + (t4 + lw) * 40960 + p * 1024, SC1);
            __builtin_amdgcn_s_waitcnt(0x0F70);
            BAR();
            bar_signal(W_F2 + (qb * 32 + head) * FL);
        }
        __builtin_amdgcn_s_setprio(0);
        STAMP(3); TR_SET(3);
        if (lw == 0) wait_flags(ws, W_F2 + (xcd * 32 + l31) * FL, tag);
        BAR();
        STAMP(4);
        {
            const int src = W_O + rb * 81920 + lw * 4096;
#pragma unroll 1
            for (int p = 0; p < 4; ++p) dmal<4>(rws, src + p * 16384, lds0 + p * 16384 + lw * 4096, vlin);
            dmal<4>(rws, src + 4 * 16384, lds0 + (lw < 2 ? 65536 : L_A9 - 8192) + lw * 4096, vlin);
            WB(16);
            copy5(W_WOT + cg * 102400 + 4 * 20480, lds0 + L_B80);
            WB(13);
            WB(0);
            BAR();
        }
        STAMP(5);
        auto dump = [&](int p) {
#pragma unroll
            for (int i = 0; i < 5; ++i) *(LAS u32x4*)(lds + ((p + 2) & 3) * P5_SLOT + 16384 + lw * 5120 + i * 1024 + vlin) = wreg[p * 5 + i];
        };
#pragma unroll
        for (int p = 0; p < 16; ++p) *(LAS u32x4*)(lds + L_XW + lw * 16384 + p * 1024 + vlin) = xreg[p];
        bar_signal(W_F3 + (rb * 8 + cg) * FL);
        TR_SET(4);
        if (lw == 0) wait_flags(ws, W_F3 + (rb * 8 + (lane & 7)) * FL, tag);
        BAR();
        STAMP(6);
#pragma unroll 1
        for (int p = 0; p < 5; ++p) dmal<4>(rws, W_H + rb * 81920 + p * 16384 + lw * 4096, lds0 + p * 16384 + lw * 4096, vlin);
        {
            u32x2 hs[4];
#pragma unroll
            for (int c = 0; c < 4; ++c) hs[c] = ld64(rws, W_HST + (rb * 16 + (lane >> 4) * 4 + c) * 512 + (lw * 16 + (lane & 15)) * 8);
            WB(20);
            WB(12);
            const float su = quad_sum(add1(add1(__uint_as_float(hs[0][0]), __uint_as_float(hs[1][0])), add1(__uint_as_float(hs[2][0]), __uint_as_float(hs[3][0]))));
            const float sq = quad_sum(add1(add1(__uint_as_float(hs[0][1]), __uint_as_float(hs[1][1])), add1(__uint_as_float(hs[2][1]), __uint_as_float(hs[3][1]))));
            const float mean = mul1(su, 1.0f / D);
            const float var = fmaxf(fma1(sq, 1.0f / D, -mean * mean), 0.f);
            float r = __builtin_amdgcn_rsqf(var + 1e-5f);
            r *= __builtin_amdgcn_rsqf(var * r * r + 1e-5f);
            const float sg = __builtin_amdgcn_rcpf(r);
            const unsigned mhs = pk2(-mean, sg), mls = pk2(add1(-mean, -bflo(mhs)), add1(sg, -bfhi(mhs)));
            *(LAS u32x4*)(lds + L_CB + (lw * 16 + (lane & 15)) * 16) = (u32x4){__float_as_uint(r), __float_as_uint(sg), mhs, mls};
            WB(0);
        }
        dump(0); dump(1);
        asm volatile("" ::: "memory");
        *(volatile LAS int*)(lds + L_CNT + 48 + lw * 4) = 1;
        STAMP(7);
        {
            const int src = W_H1 + rb * 221184 + lw * 4096;
            auto issueA = [&](int p) { dmal<4>(rws, src + (p < 8 ? (p + cg) & 7 : p) * 16384, lds0 + ((p + 2) & 3) * P5_SLOT + lw * 4096, vlin); };
            auto issueB = [&](int p) { copy5(w2s + (p < 8 ? (p + cg) & 7 : p) * 20480, lds0 + ((p + (RB ? -1 : 2)) & 3) * P5_SLOT + 16384); };
            wait_lds<1>(lds + L_CNT + min(lane, cg < 6 ? 6 : 5) * 4, 1);
            dump(2); dump(3);
            wait_flags(ws, W_F4 + (rb * 64 + (lane & 7) * 4 + lw) * FL, tag);
            STAMP(8);
#pragma unroll 1
            for (int p = OWN; p < 4; ++p) issueA(p);
            TR_SET(5);
            if (!OWN) WB(12);
            WB(8); issueA(4); issueB(4);
            WB(13); issueA(5); issueB(5);
            WB(18); issueA(6); issueB(6);
            WB(18);
            const unsigned fl = L_CB + 2048 + lw * 256;
            const int fb = W_F4 + (rb * 64 + 32 + lw) * FL, nb = lw < 2 ? 5 : 4;
            touch(rws, fb, lds0 + fl, min(lane >> 1, nb) * 4 * FL + (lane & 1) * 4);
            issueA(7); issueB(7);
            WB(9);
            if (!__all(*(LAS u64*)(lds + fl + l31 * 8) == tag)) wait_flags(ws, fb + min(lane, nb) * 4 * FL, tag);
            issueA(8); issueB(8);
            WB(18); issueA(9); issueB(9);
#pragma unroll 1
            for (int p = 10; p <= 12; ++p) { WB(18); issueA(p); issueB(p); }
            WB(18);
            if (lw < 2) { issueA(13); issueB(13); WB(18); WB(9); } else { WB(9); WB(0); }
            WB(0);
        }
        STAMP(9); TR_DUMP();
        return;
    }

    const rsrc_t rx = __builtin_amdgcn_make_buffer_rsrc((void*)x, 0, S * D * 2, 0x00020000);
    const int xo = lane < 40 ? lane * 32 : S * D * 2;
    const u32x4 xa_ = __builtin_amdgcn_raw_buffer_load_b128(rx, xo, (m0 + nt * 8 + w) * (D * 2), 0);
    const u32x4 xb_ = __builtin_amdgcn_raw_buffer_load_b128(rx, xo + 16, (m0 + nt * 8 + w) * (D * 2), 0);
    const u32x2 nonce = ld64(rws, w == 0 ? W_NONCE : W_END);
    if (w == 6) {
        const rsrc_t rc = __builtin_amdgcn_make_buffer_rsrc((void*)pc, 0, CODE_BYTES, 0x00020000);
        touch(rc, (jx & 7) * 8192, lds0 + L_P1SCR + 29696, lane * 128);
    }
    auto unit_lines = [&](lds_ptr srow) {
        const unsigned c = lane == 8 ? 0x3f803f80u : 0u;
        const u32x4 v = lds128(srow + (lane & 3) * 16);
        if (lane < 16) st128(rws, lane < 8 ? W_RS + (mt * 16 + nt) * 128 + lane * 16 : W_VT + (mt * 16 + nt) * VP + 10240 - 128 + lane * 16, lane < 8 ? v : (u32x4){c, c, c, c});
    };
    if (gemm) {
        const int wm = w >> 2, wn = w & 3;
        unsigned cv[5];
#pragma unroll
        for (int r = 0; r < 5; ++r) cv[r] = *(const unsigned*)(wqkv + (mt * 40 + w * 5 + r) * (3 * D * 2) + n0 * 2 + lane * 4);
        TRC();
        unsigned aoff[4];
#pragma unroll
        for (int ks = 0; ks < 4; ++ks) aoff[ks] = (wm * 64 + l31) * 128 + (((2 * ks + hh) ^ ((lane >> 1) & 7)) << 4);
        const unsigned boff = 16384 + 2 * hh * 1024 + wn * 256 + ((lane & 15) >> 2) * 64 + ((lane >> 4) & 1) * 32 + (lane & 3) * 8;
        const bool vtile = nt >= 10, isq = nt < 5;
        int dst[2], tstr[2];
        {
            const int kt = (m0 + wm * 64) >> 5;
            if (!vtile) {
#pragma unroll
                for (int g = 0; g < 2; ++g) {
                    const int G = (n0 - (isq ? 0 : D) + wn * 32) / 8 + 2 * g + hh, pr = (G * 205) >> 10, r5 = G - pr * 5;
                    tstr[g] = r5 == 2 ? 512 : 1024;
                    dst[g] = (r5 == 2 ? (isq ? W_QR : W_KR) + pr * 32768 : (isq ? W_QM : W_KM) + (2 * pr + (r5 > 2)) * 65536 + (r5 > 2 ? r5 - 3 : r5) * 512)
                             + kt * tstr[g] + l31 * 16;
                }
            } else {
                dst[0] = W_VT + (kt * 4 + hh) * VP + (n0 - 2 * D + wn * 32 + l31) * 16; dst[1] = dst[0] + 2 * VP; tstr[0] = tstr[1] = 4 * VP;
            }
            PIN(dst[0]); PIN(dst[1]); PIN(tstr[0]); PIN(tstr[1]);
        }
        f32x16 acc[2] = {f32x16{}, f32x16{}};
        PIN(aoff[0]); PIN(aoff[1]); PIN(aoff[2]); PIN(aoff[3]);
        SBAR();
        {
            const f32x2 st = row_stats(xa_, xb_);
            f32x2 c = {};
#pragma unroll
            for (int r = 0; r < 5; ++r) { c[0] += bflo(cv[r]); c[1] += bfhi(cv[r]); }
            *(LAS f32x2*)(lds + L_P1SCR + w * 512 + lane * 8) = c;
            if (lane < 2) *(LAS float*)(lds + L_P1SCR + 24576 + lane * 32 + w * 4) = lane ? st[1] : st[0];
            if (tid == 0) *(LAS u64*)(lds + L_TAG) = __builtin_bit_cast(u64, nonce) + 1;
        }
        BAR();
        const u64 tag = uni64(*(LAS u64*)(lds + L_TAG));
        if (w == 7) {
            f32x2 c = {};
#pragma unroll
            for (int i = 0; i < 8; ++i) c += *(LAS f32x2*)(lds + L_P1SCR + i * 512 + lane * 8);
            st64(rws, W_CSP + (mt * 1920 + n0) * 4 + lane * 8, __builtin_bit_cast(u32x2, c));
            unit_lines(lds + L_P1SCR + 24576);
        }
        const int tpub = w == 7 ? 1 : -1;
        auto hook = [&](int t) {
            if (t == tpub) {
                DRAIN();
                if (lane < FW) set_flag(ws, W_F0 + (mt * 16 + nt) * FL + lane * 8, tag);
            }
        };
        lds_ptr srs = lds + L_P1SCR + 12288 + wm * 256, scs = lds + L_P1SCR + 13312 + wn * 128;
        auto put = [&](int i, int g, const float (&v)[8]) {
            st128(rws, dst[g] + i * tstr[g], widen((u32x2){pk2(v[0], v[1]), pk2(v[2], v[3])}, (u32x2){pk2(v[4], v[5]), pk2(v[6], v[7])}));
            SBAR();
        };
        if (!vtile) {
            f32x4 c4[4]; float mean[2], r[2];
            loop32<true>(lds, aoff, boff, acc, hook, [&]() {
#pragma unroll
                for (int g = 0; g < 4; ++g) c4[g] = *(LAS const f32x4*)(scs + (g * 8 + hh * 4) * 4);
#pragma unroll
                for (int i = 0; i < 2; ++i) { mean[i] = *(LAS float*)(srs + (i * 32 + l31) * 4); r[i] = *(LAS float*)(srs + 512 + (i * 32 + l31) * 4); }
            } TR_PASS);
            STAMP(1);
#pragma unroll
            for (int q = 0; q < 4; ++q) {
                const int i = q >> 1, g = q & 1;
                const float rs = r[i] * (isq ? 0.22360679774997896f * 1.4426950408889634f : 1.0f);
                float v[8];
#pragma unroll
                for (int e = 0; e < 8; ++e) v[e] = rs * (acc[i][8 * g + e] - mean[i] * c4[2 * g + (e >> 2)][e & 3]);
                put(i, g, v);
            }
        } else {
            float cs; f32x4 m4[8], r4[8];
            loop32<false>(lds, aoff, boff, acc, hook, [&]() {
                cs = *(LAS float*)(scs + l31 * 4);
#pragma unroll
                for (int g = 0; g < 8; ++g) { m4[g] = *(LAS const f32x4*)(srs + (g * 8 + hh * 4) * 4); r4[g] = *(LAS const f32x4*)(srs + 512 + (g * 8 + hh * 4) * 4); }
            } TR_PASS);
            STAMP(1);
#pragma unroll
            for (int q = 0; q < 4; ++q) {
                float v[8];
#pragma unroll
                for (int e = 0; e < 8; ++e) v[e] = r4[2 * q + (e >> 2)][e & 3] * (acc[q >> 1][8 * (q & 1) + e] - m4[2 * q + (e >> 2)][e & 3] * cs);
                put(q >> 1, q & 1, v);
            }
        }
        DRAIN(); BAR();
    } else {
        const f32x2 st = row_stats(xa_, xb_);
        const u64 tag = __builtin_bit_cast(u64, nonce) + 1;
        if (lane < 2) *(LAS float*)(lds + L_P1SCR + 24576 + lane * 32 + w * 4) = lane ? st[1] : st[0];
        if (tid == 0) *(LAS u64*)(lds + L_TAG) = tag;
        BAR();
        if (w == 0) {
            unit_lines(lds + L_P1SCR + 24576);
            DRAIN();
            if (lane < FW) set_flag(ws, W_F0 + (mt * 16 + 15) * FL + lane * 8, tag);
        }
        const rsrc_t r1 = __builtin_amdgcn_make_buffer_rsrc((void*)w1, 0, D * FF * 2, 0x00020000);
        const int col = mt * 128 + (tid & 15) * 8, vo = col < FF ? (tid >> 4) * (FF * 2) + col * 2 : D * FF * 2;
        float cs[8] = {};
#pragma unroll 10
        for (int r = 0; r < 20; ++r) {
            const u32x4 v = __builtin_amdgcn_raw_buffer_load_b128(r1, vo, r * 32 * (FF * 2), 0);
#pragma unroll
            for (int e = 0; e < 4; ++e) { cs[2 * e] += bflo(v[e]); cs[2 * e + 1] += bfhi(v[e]); }
        }
        *(LAS f32x4*)(lds + tid * 32) = (f32x4){cs[0], cs[1], cs[2], cs[3]};
        *(LAS f32x4*)(lds + tid * 32 + 16) = (f32x4){cs[4], cs[5], cs[6], cs[7]};
        BAR();
        if (w == 0) {
            f32x2 t = {};
#pragma unroll 8
            for (int r = 0; r < 32; ++r) t += *(LAS f32x2*)(lds + r * 512 + lane * 8);
            st64(rws, W_CS1 + mt * 512 + lane * 8, __builtin_bit_cast(u32x2, t));
            DRAIN();
            if (lane < FW) set_flag(ws, W_FW + mt * FL + lane * 8, tag);
        }
    }
    STAMP(2); TR_SET(2);
    const u64 tag = uni64(*(LAS u64*)(lds + L_TAG));
    {
        const int wq = w & 3, kh = w >> 2;
        const int q0 = qb * 256 + wq * 64;
        BAR();
        u32x4 qm[2]; u32x2 qe[2], qo[2];
#pragma unroll
        for (int i = 0; i < 2; ++i) {
            qm[i] = ld128(rws, W_QM + head * 65536 + ((q0 >> 5) + (i ^ kh)) * 1024 + lane * 16);
            const u32x2 qr = ld64(rws, W_QR + (head >> 1) * 32768 + (q0 + (i ^ kh) * 32 + l31) * 16 + (head & 1) * 8);
            qe[i] = hh == 0 ? qr : (u32x2){0u, 0u}; qo[i] = hh == 1 ? qr : (u32x2){0u, 0u};
        }
        const unsigned vrow = l31 < 20 ? (hh * 20 + l31) * 16 : (l31 == 20 ? 640 : 656);
        const unsigned koff = lds0 + lane * 16 + kh * 4096, roff = lds0 + 8192 + lane * 16 + (head & 1) * 8 + kh * 2048, voff = lds0 + 12288 + kh * 4 * 1408 + vrow;
        unsigned idx = 0xddddddddu; PIN(idx);
        f32x16 s0, s1, nm0, nm1, o0 = {}, o1 = {};
        u32x4 ka_, kb_, vla, vha, vlb = {}, vhb = {}; u32x2 kra, krb;
        PIN(o0); PIN(o1); PIN(vhb); PIN(vlb);
        BAR();
        STAMP(3);
        {
            lds_ptr c0 = (lds_ptr)(size_t)(3 * AT_SLOT);
            kb_ = lds128(lds + 3 * AT_SLOT + lane * 16); krb = lds64(lds + 3 * AT_SLOT + 8192 + lane * 16 + (head & 1) * 8);
            ka_ = lds128(c0 + koff); kra = lds64(c0 + roff); vla = lds128(c0 + voff); vha = lds128(c0 + voff + 704);
            s0 = mfma8(krb, qe[0], mfma32(kb_, qm[0], f32x16{}));
            s1 = mfma8(krb, qe[1], mfma32(kb_, qm[1], f32x16{}));
            float a = s0[0], b = s1[0];
#pragma unroll
            for (int r = 1; r < 16; ++r) { a = fmaxf(a, s0[r]); b = fmaxf(b, s1[r]); }
            a = -half_max(a); b = -half_max(b);
#pragma unroll
            for (int r = 0; r < 16; ++r) { nm0[r] = a; nm1[r] = b; }
            PIN(nm0); PIN(nm1);
        }
        s0 = mfma8(kra, qe[0], mfma32(ka_, qm[0], nm0));
        s1 = f32x16{};
        asm volatile("s_nop 15" : "+v"(s0), "+v"(s1));
        {
            unsigned ka = koff + 3 * AT_SLOT, ra = roff + 3 * AT_SLOT, va = voff + 3 * AT_SLOT, pv, tv;
            int c = 0, t0, t1, td;
            asm volatile(ATT_LOOP
                         : ATT_IO, [ka] "+v"(ka), [ra] "+v"(ra), [va] "+v"(va), [pv] "=&v"(pv), [t] "=&v"(tv), [c] "+s"(c), [s0] "=&s"(t0), [s1] "=&s"(t1), [d] "=&s"(td)
                         : ATT_IN, [wa] "v"(lds0 + L_FULL), [pa] "v"(lds0 + L_FULL + 32 + (w ^ 4) * 4), [ma] "v"(lds0 + L_FULL + 32 + w * 4),
                           [kh] "s"(kh), [st] "s"(AT_SLOT), [w5] "s"(-5 * AT_SLOT), [w2] "s"(-2 * AT_SLOT)
                         : "memory", "scc", "v22", "v23");
        }
        asm volatile("s_nop 1" : "+v"(s1));
        o1 = smfma(vhb, hi8(s1), o1, idx);
        SBAR();
        wait_lds<4>(lds + L_FULL + 32 + (lane & 7) * 4, 6);
        asm volatile("s_nop 7" : "+v"(o0), "+v"(o1));
        STAMP(4);
        {
            lds_ptr ex = lds + 2 * AT_SLOT + lane * 16;
#pragma unroll
            for (int g = 0; g < 3; ++g)
                *(LAS f32x4*)(ex + w * 3072 + g * 1024) = (f32x4){o1[4 * g], o1[4 * g + 1], o1[4 * g + 2], o1[4 * g + 3]};
            const int ob = W_O + (q0 >> 6) * 81920;
            int d0 = ob + (head * 2 + hh) * 1024 + (kh * 32 + l31) * 16, d1 = hh ? W_END : ob + 65536 + head * 512 + (((kh * 32 + l31) * 8) ^ ((head & 2) << 6));
            unsigned er = (unsigned)(size_t)ex + (w ^ 4) * 3072;
            PIN(d0); PIN(d1); PIN(er);
            auto put_o = [&](const float (&o)[12]) {
                auto lr = __builtin_amdgcn_permlane32_swap(__float_as_uint(o[8]), __float_as_uint(o[8]), false, false);
                const float inv = __builtin_amdgcn_rcpf(__uint_as_float(lr[1]));
                unsigned pk[6];
#pragma unroll
                for (int e = 0; e < 6; ++e) pk[e] = pk2(o[2 * e] * inv, o[2 * e + 1] * inv);
                st128(rws, d0, widen((u32x2){pk[0], pk[1]}, (u32x2){pk[2], pk[3]}));
                st64(rws, d1, (u32x2){pk[4], pk[5]});
            };
            BAR();
            float o[12];
            {
                f32x4 p[3];
#pragma unroll
                for (int g = 0; g < 3; ++g) p[(g + 2) % 3] = *(LAS f32x4*)(size_t)(er + ((g + 2) % 3) * 1024);
                SBAR();
#pragma unroll
                for (int e = 0; e < 12; ++e) o[e] = p[e >> 2][e & 3] + o0[e];
            }
            put_o(o);
            SBAR();
            float chk = 0.f;
#pragma unroll
            for (int e = 0; e < 12; ++e) chk += fabsf(o[e]);
            if (__any(!(chk < 1e30f))) {
                const u32x4 fq = qm[0]; const u32x2 fe = qe[0], fo = qo[0];
                f32x16 fn = {}, fa = {};
#pragma unroll 1
                for (int pass = 0; pass < 2; ++pass) {
                    float m = -INFINITY;
#pragma unroll 1
                    for (int t = 0; t < 64; ++t) {
                        const u32x4 a = ld128(rws, W_KM + head * 65536 + t * 1024 + lane * 16);
                        const u32x2 b = ld64(rws, W_KR + (head >> 1) * 32768 + (t >> 1) * 1024 + lane * 16 + (head & 1) * 8);
                        f32x16 s = mfma8(b, (t & 1) ? fo : fe, mfma32(a, fq, fn));
                        if (pass == 0) {
#pragma unroll
                            for (int r = 0; r < 16; ++r) m = fmaxf(m, s[r]);
                        } else {
#pragma unroll
                            for (int r = 0; r < 16; ++r) s[r] = __builtin_amdgcn_exp2f(s[r]);
                            const int vo = W_VT + (t * 4 + hh) * VP + (l31 < 20 ? head * 20 + l31 : l31 == 20 ? 640 : 641) * 16;
                            fa = smfma(ld128(rws, vo), lo8(s), fa, idx);
                            fa = smfma(ld128(rws, vo + 2 * VP), hi8(s), fa, idx);
                        }
                    }
                    m = -half_max(m);
#pragma unroll
                    for (int r = 0; r < 16; ++r) fn[r] = m;
                }
#pragma unroll
                for (int r = 0; r < 12; ++r) o[r] = fa[r];
                DRAIN();
                put_o(o);
            }
            DRAIN(); BAR();
        }
    }
    STAMP(5); TR_SET(3);

    const int l15 = lane & 15, kg = lane >> 4;
    const bool act = w < (cg < 6 ? 7 : 6);
    const int w1s = act ? W_W1T + (t4 + w) * 40960 : W_END;
    constexpr int WIN = 20;
    auto w2a = [&](int f) { return W_W2T + cg * 276480 + ((2 * (f / 5) + cg) & 7) * 20480 + (w >> 1) * 5120 + (f % 5) * 1024; };
    auto ldf = [&](int so) { return __builtin_amdgcn_raw_buffer_load_b128(rws, vlin, so, SC1); };
    unsigned e0, e1;
    {
        const int nw = (t4 + (act ? w : 0)) * 32;
        const float c = ld32f(rws, W_CS1 + (nw + l31) * 4);
        e0 = pk2(c, c); e1 = (pk2(c - bflo(e0), 0.f) & 0xffffu) | ((unsigned)*(const uint16_t*)(b1 + (nw + l31) * 2) << 16);
        if (hh) e0 = e1 = 0u;
        PIN(e0); PIN(e1);
    }
    unsigned hp[6];
    {
        const int mi = w & 3, nh = w >> 2;
        auto wo_phase = [&](auto njc) {
            constexpr int NJ = decltype(njc)::value;
            u32x2 xr[NJ];
#pragma unroll
            for (int j = 0; j < NJ; ++j) xr[j] = *(const u32x2*)(x + (rb * 64 + mi * 16 + l15) * (D * 2) + (cg * 80 + nh * 48 + j * 16 + kg * 4) * 2);
            f32x4 acc[NJ];
            int hb = W_H + rb * 81920 + (cg * 10 + nh * 6 + (kg >> 1)) * 1024 + (mi * 16 + l15) * 16;
            int h0 = hb + (kg & 1) * 2048, h1 = hb + 4096 + (kg & 1) * 8, h2 = kg == 0 ? W_HST + (rb * 8 + cg) * 1024 + nh * 512 + (mi * 16 + l15) * 8 : W_END;
            PIN(h0); PIN(h1); PIN(h2);
            constexpr int KP = NJ == 3 ? KP3 : KP2;
            u32x4 xa[20], wf[20][NJ];
            lds_ptr ap = lds + kg * 1024 + (mi * 16 + l15) * 16, bp0 = lds + L_B80 + nh * 3072 + vlin, bp1 = bp0 + 40960;
            lds_ptr ar = lds + kg * 1024 + (((mi * 16 + l15) * 8) ^ ((kg & 1) << 7)), ar8 = ar + 65536, ar9 = ar + L_A9;
            auto rda = [&](int k) {
                if (k < 16) xa[k] = lds128(ap + k * 4096);
                else { lds_ptr q = (k < 18 ? ar8 : ar9) + (k & 1) * 4096; xa[k] = cat(lds64(q), lds64(q + 512)); }
            };
            auto rdb = [&](int k) {
                lds_ptr bp = ((k & 15) < 8 ? bp0 : bp1) + (k & 7) * 5120;
#pragma unroll
                for (int j = 0; j < NJ; ++j) wf[k][j] = lds128(bp + j * 1024);
            };
#pragma unroll
            for (int k = 0; k < KP; ++k) rdb(k);
#pragma unroll
            for (int j = 0; j < NJ; ++j) { acc[j] = (f32x4){bflo(xr[j][0]), bfhi(xr[j][0]), bflo(xr[j][1]), bfhi(xr[j][1])}; PIN(acc[j]); }
            BAR();
            BARN();
            rda(0); rda(1); rda(2); rda(3);
#pragma unroll
            for (int k = 0; k < 20; ++k) {
                if (k == 1) BARN();
                if (k == 9) BARN();
                if (k == 18) BAR();
                acc[0] = mfma16(wf[k][0], xa[k], acc[0]); SBAR();
                if (k >= 1 && k + 3 < 20) rda(k + 3);
                if (k + BD >= KP && k + BD < 20) rdb(k + BD);
                SBAR();
#pragma unroll
                for (int j = 1; j < NJ; ++j) { acc[j] = mfma16(wf[k][j], xa[k], acc[j]); SBAR(); }
            }
            STAMP(6);
#pragma unroll
            for (int e = 0; e < 6; ++e) hp[e] = e < 2 * NJ ? pk2(acc[(e >> 1) % NJ][2 * (e & 1)], acc[(e >> 1) % NJ][2 * (e & 1) + 1]) : 0u;
            const auto r0 = __builtin_amdgcn_permlane16_swap(hp[0], hp[2], false, false);
            const auto r1 = __builtin_amdgcn_permlane16_swap(hp[1], hp[3], false, false);
            st128(rws, h0, (u32x4){r0[0], r1[0], r0[1], r1[1]});
            if (NJ == 3) st64(rws, h1, (u32x2){hp[4], hp[5]});
            SBAR();
            f32x4 s4 = acc[0] + acc[1], q4 = acc[0] * acc[0] + acc[1] * acc[1];
            if (NJ == 3) { s4 += acc[NJ - 1]; q4 += acc[NJ - 1] * acc[NJ - 1]; }
            const float su = quad_sum((s4[0] + s4[1]) + (s4[2] + s4[3])), sq = quad_sum((q4[0] + q4[1]) + (q4[2] + q4[3]));
            st64(rws, h2, (u32x2){__float_as_uint(su), __float_as_uint(sq)});
        };
        if (nh == 0) wo_phase(std::integral_constant<int, 3>{}); else wo_phase(std::integral_constant<int, 2>{});
        if (tid < 16) *(LAS unsigned*)(lds + L_CNT + tid * 4) = 0u;
        DRAIN(); BAR();
    }
    STAMP(7); TR_SET(4);
    const int kp = kpos(cg, w);
    if (!act) {
        BAR(); BAR(); BARN(); BARN();
    } else {
        u32x4 wb[WIN];
        if (w < 4) {
#pragma unroll
            for (int k = 0; k < 16; ++k) wb[k] = lds128(lds + L_XW + w * 16384 + k * 1024 + vlin);
            BAR();
            BAR();
            __builtin_amdgcn_s_setprio(2);
        } else {
            BAR(); BAR(); BARN(); BARN();
#pragma unroll
            for (int k = 0; k < 16; ++k) wb[k] = ldf(w1s + k * 1024);
            __builtin_amdgcn_s_setprio(1);
        }
#pragma unroll
        for (int k = 16; k < WIN; ++k) wb[k] = ldf(w1s + k * 1024);
        f32x16 acc[2] = {f32x16{}, f32x16{}};
        lds_ptr ap = lds + hh * 1024 + l31 * 16, ap2 = ap + 40960;
        u32x4 xa[4][2], st[2];
#pragma unroll
        for (int k = 0; k < 2; ++k) { xa[k][0] = lds128(ap + k * 2048); xa[k][1] = lds128(ap + k * 2048 + 512); }
#pragma unroll
        for (int k = 0; k < 40; ++k) {
            if ((k == 6 || k == KC) && w < 4) BARN();
            if (k == 28) { st[0] = lds128(lds + L_CB + l31 * 16); st[1] = lds128(lds + L_CB + 512 + l31 * 16); SBAR(); }
            if (k == 32) {
#pragma unroll
                for (int i = 0; i < 2; ++i)
                    acc[i] = mfma32((u32x4){e0, e1, e1 >> 16, 0u}, (u32x4){(st[i][2] & 0xffffu) | (st[i][3] << 16), st[i][2], st[i][3] >> 16, 0u}, acc[i]);
                SBAR();
            }
            lds_ptr np = k + 2 < 20 ? ap + (k + 2) * 2048 : ap2 + (k - 18) * 2048;
            acc[0] = mfma32(wb[k % WIN], xa[k & 3][0], acc[0]); SBAR();
            if (k + 2 < 40) { xa[(k + 2) & 3][0] = lds128(np); xa[(k + 2) & 3][1] = lds128(np + 512); SBAR(); }
            acc[1] = mfma32(wb[k % WIN], xa[k & 3][1], acc[1]); SBAR();
            if (k < WIN) wb[k] = __builtin_amdgcn_raw_buffer_load_b128(rws, vlin, w1s + (k + WIN) * 1024, SC1);
            SBAR();
        }
        STAMP(8); TRC();
        *(volatile LAS int*)(lds + L_CNT + w * 4) = 1;
        asm volatile("s_nop 15" : "+v"(acc[0]), "+v"(acc[1]));
#pragma unroll
        for (int i = 0; i < 2; ++i) {
            const f32x16 a = acc[i];
            const float r = __uint_as_float(st[i][0]), sg = __uint_as_float(st[i][1]);
            const float k1 = mul1(r, -2.302208198f), k3 = mul1(mul1(r, r), mul1(r, -0.1029432397f));
            float y[16]; unsigned pk[8];
#pragma unroll
            for (int e = 0; e < 16; ++e) y[e] = mul1(a[e], a[e]);
#pragma unroll
            for (int e = 0; e < 16; ++e) y[e] = fma1(y[e], k3, k1);
#pragma unroll
            for (int e = 0; e < 16; ++e) y[e] = mul1(y[e], a[e]);
            SBAR();
#pragma unroll
            for (int e = 0; e < 16; ++e) y[e] = __builtin_amdgcn_exp2f(y[e]);
            SBAR();
#pragma unroll
            for (int e = 0; e < 16; ++e) y[e] = fma1(y[e], sg, sg);
            SBAR();
#pragma unroll
            for (int e = 0; e < 16; ++e) y[e] = __builtin_amdgcn_rcpf(y[e]);
            SBAR();
#pragma unroll
            for (int e = 0; e < 16; ++e) y[e] = mul1(y[e], a[e]);
            SBAR();
#pragma unroll
            for (int e = 0; e < 8; ++e) pk[e] = pk2(y[2 * e], y[2 * e + 1]);
#pragma unroll
            for (int g = 0; g < 2; ++g)
            {
                const u32x4 u = widen((u32x2){pk[4 * g], pk[4 * g + 1]}, (u32x2){pk[4 * g + 2], pk[4 * g + 3]});
                st128(rws, W_H1 + rb * 221184 + kp * 4096 + (2 * g + hh) * 1024 + (i * 32 + l31) * 16, u);
                if (OWN && w < 4) *(LAS u32x4*)(lds + 2 * P5_SLOT + w * 4096 + (2 * g + hh) * 1024 + (i * 32 + l31) * 16) = u;
            }
            SBAR();
        }
        TRC();
        if (w < 4) {
            asm volatile("" ::: "memory");
            *(volatile LAS int*)(lds + L_CNT + 32 + w * 4) = 1;
            DRAIN();
            if (lane < FW) set_flag(ws, W_F4 + (rb * 64 + kp) * FL + lane * 8, tag);
        }
    }
    __builtin_amdgcn_s_setprio(0);
    if (blockIdx.x == 0 && tid == 0) set_flag(ws, W_NONCE, tag);
    STAMP(9); TR_SET(5);
    {
        const int mi = w & 3, nh = w >> 2;
        char* outp = out; asm volatile("" : "+s"(outp));
        const rsrc_t ro = __builtin_amdgcn_make_buffer_rsrc(outp, 0, S * D * 2, 0x00020000);
        auto w2_phase = [&](auto njc) {
            constexpr int NJ = decltype(njc)::value;
            f32x4 acc[NJ]; u32x2 bv[NJ];
#pragma unroll
            for (int j = 0; j < NJ; ++j) {
                bv[j] = *(const u32x2*)(b2 + (cg * 80 + nh * 48 + j * 16 + kg * 4) * 2);
                acc[j] = (f32x4){bflo(hp[2 * j]), bfhi(hp[2 * j]), bflo(hp[2 * j + 1]), bfhi(hp[2 * j + 1])};
            }
            const unsigned ao = kg * 1024 + (mi * 16 + l15) * 16, bo = 16384 + nh * 3072 + vlin;
            int o0 = (rb * 64 + mi * 16 + l15) * (D * 2) + (cg * 80 + nh * 48) * 2, o1 = o0 + 64 + kg * 8;
            o0 += (kg & 1) * 32 + (kg >> 1) * 16;
            PIN(o0); PIN(o1);
            u32x4 xa[4], wf[4][NJ];
            auto half = [&](int m, int r, int sl, bool domm, bool dord) {
                lds_ptr ap = lds + sl * P5_SLOT + ao, bp = lds + sl * P5_SLOT + bo;
#pragma unroll
                for (int q = 0; q < 2 * NJ; ++q) {
                    if (domm) { acc[q % NJ] = mfma16(wf[m + q / NJ][q % NJ], xa[m + q / NJ], acc[q % NJ]); SBAR(); }
                    if (dord) {
#pragma unroll
                        for (int e = 2 * q; e < 2 * q + 2 && e < 2 * NJ + 2; ++e) {
                            const int k = r + e / (NJ + 1), f = e % (NJ + 1);
                            if (f == 0) xa[k] = lds128(ap + k * 4096); else wf[k][f - 1] = lds128(bp + k * 5120 + (f - 1) * 1024);
                        }
                        SBAR();
                    }
                }
            };
#pragma unroll
            for (int j = 0; j < NJ; ++j) PIN(acc[j]);
            __builtin_amdgcn_s_waitcnt(0xC07F);
            if (OWN) wait_lds<1>(lds + L_CNT + 32 + (lane & 7) * 4, 1); else BAR();
            half(0, 0, 2, false, true);
            const int ppub = (act && NJ == 2) ? YPUB : -1;
#pragma unroll 1
            for (int p = 0; p < 13; ++p) {
                half(0, 2, (p + 2) & 3, true, true);
                if (p == ppub) {
                    DRAIN();
                    if (lane < FW) set_flag(ws, W_F4 + (rb * 64 + kp) * FL + lane * 8, tag);
                }
                BAR();
                half(2, 0, (p + 3) & 3, true, true);
            }
            half(0, 0, 0, true, false);
            STAMP(10);
            unsigned ov[2 * NJ];
#pragma unroll
            for (int j = 0; j < NJ; ++j) {
                ov[2 * j] = pk2(acc[j][0] + bflo(bv[j][0]), acc[j][1] + bfhi(bv[j][0]));
                ov[2 * j + 1] = pk2(acc[j][2] + bflo(bv[j][1]), acc[j][3] + bfhi(bv[j][1]));
            }
            const auto r0 = __builtin_amdgcn_permlane16_swap(ov[0], ov[2], false, false);
            const auto r1 = __builtin_amdgcn_permlane16_swap(ov[1], ov[3], false, false);
            __builtin_amdgcn_raw_buffer_store_b128((u32x4){r0[0], r1[0], r0[1], r1[1]}, ro, o0, 0, 0);
            if (NJ == 3) __builtin_amdgcn_raw_buffer_store_b64((u32x2){ov[2 * NJ - 2], ov[2 * NJ - 1]}, ro, o1, 0, 0);
        };
        if (nh == 0) w2_phase(std::integral_constant<int, 3>{}); else w2_phase(std::integral_constant<int, 2>{});
    }
    STAMP(11); TR_DUMP();
}
}

extern "C" void kernel_launch(void* const* d_in, const int* in_sizes, int n_in, void* d_out, int out_size, void* d_ws, size_t ws_size,
                              hipStream_t stream) {
    (void)hipFuncSetAttribute((const void*)k_block, hipFuncAttributeMaxDynamicSharedMemorySize, LDS_BYTES);
    k_block<<<256, 768, LDS_BYTES, stream>>>((const char*)d_in[0], (const char*)d_in[1], (char*)d_ws, (const char*)d_in[4], (const char*)d_in[2],
                                             (const char*)d_in[6], (const char*)d_in[5], (const char*)d_in[7], (char*)d_out);
}
```
